```python
import torch

def _softmax(x, axis=-1):
    return torch.softmax(x, dim=axis)
def _gelu(x):
    return torch.nn.functional.gelu(x, approximate="tanh")
def _silu(x):
    return torch.nn.functional.silu(x)
def _sigmoid(x):
    return torch.sigmoid(x)
def _layernorm(x, axis=-1, eps=1e-5):
    m = x.mean(dim=axis, keepdim=True)
    v = x.var(dim=axis, keepdim=True, unbiased=False)
    return (x - m) / torch.sqrt(v + eps)
def _scatter_add(tgt, idx, src, axis=0):
    return tgt.clone().index_add_(axis, idx, src)
def _segment_sum(src, seg, n):
    out = torch.zeros((n,) + src.shape[1:], dtype=src.dtype)
    return out.index_add_(0, seg, src)
def _pad(x, pw):
    flat = tuple(v for pair in reversed(pw) for v in pair)
    return torch.nn.functional.pad(x, flat)

kernel_name = 'parallel_sum__gqa_block_cached__glu_gated_bilinear__decode'

def setup_inputs():

    _w = (lambda a: (lambda t: t.to(torch.bfloat16) if t.is_floating_point() else t)(torch.as_tensor(a)))
    def _A_setup():
        B, Sq, D = 2, 1, 576
        Hq, Hkv, Sk, Dh = 32, 16, 4096, 128
        assert Hq % Hkv == 0, (Hq, Hkv)
        return {
            "x": _w((lambda *s: torch.randn(*s, dtype=torch.bfloat16) * 0.05)(B, Sq, D)),
            "k_cache": _w((lambda *s: torch.randn(*s, dtype=torch.bfloat16) * 1.6)(B, Hkv, Sk, Dh)),
            "v_cache": _w((lambda *s: torch.randn(*s, dtype=torch.bfloat16) * 0.8)(B, Hkv, Sk, Dh)),
            "wq": _w((lambda *s: torch.randn(*s, dtype=torch.bfloat16) * 1.6)(D, Hq * Dh) * (D ** -0.5)),
            "wo": _w((lambda *s: torch.randn(*s, dtype=torch.bfloat16) * 0.8)(Hq * Dh, D) * ((Hq * Dh) ** -0.5)),
        }
    _A_in = _A_setup()

    def _B_setup():
        B, S, D, F = 2, 1, 576, 4608
        return {
            "x": _w((lambda *s: torch.randn(*s, dtype=torch.bfloat16) * 0.05)(B, S, D)),
            "w_gate": _w((lambda *s: torch.randn(*s, dtype=torch.bfloat16) * 0.4)(D, F) * (D ** -0.5)),
            "w_up": _w((lambda *s: torch.randn(*s, dtype=torch.bfloat16) * 0.4)(D, F) * (D ** -0.5)),
            "w_down": _w((lambda *s: torch.randn(*s, dtype=torch.bfloat16) * 0.4)(F, D) * (F ** -0.5)),
        }
    _B_in = _B_setup()

    return {
        "A_x": _A_in["x"],
        "A_k_cache": _A_in["k_cache"],
        "A_v_cache": _A_in["v_cache"],
        "A_wq": _A_in["wq"],
        "A_wo": _A_in["wo"],
        "B_x": _B_in["x"],
        "B_w_gate": _B_in["w_gate"],
        "B_w_up": _B_in["w_up"],
        "B_w_down": _B_in["w_down"],
    }

def reference(A_x, A_k_cache, A_v_cache, A_wq, A_wo, B_x, B_w_gate, B_w_up, B_w_down):
    def _A_ref(x, k_cache, v_cache, wq, wo):
        B, Sq, D = x.shape
        Hq, Dh = 32, 128
        Hkv = k_cache.shape[1]
        rep = Hq // Hkv
        q = (lambda x, *d: x.permute(*d))((x @ wq).reshape(B, Sq, Hq, Dh), 0, 2, 1, 3)
        k = (lambda x, n, axis: torch.repeat_interleave(x, n, dim=axis))(k_cache, rep, axis=1)
        v = (lambda x, n, axis: torch.repeat_interleave(x, n, dim=axis))(v_cache, rep, axis=1)
        scores = torch.einsum("bhqd,bhkd->bhqk", q, k) * (Dh ** -0.5)
        p = _softmax(scores, axis=-1)
        o = torch.einsum("bhqk,bhkd->bhqd", p, v)
        return (lambda x, *d: x.permute(*d))(o, 0, 2, 1, 3).reshape(B, Sq, Hq * Dh) @ wo

    def _B_ref(x, w_gate, w_up, w_down):
        g = torch.einsum("bsd,df->bsf", x, w_gate)
        u = torch.einsum("bsd,df->bsf", x, w_up)
        h = g * u
        return torch.einsum("bsf,fd->bsd", h, w_down)

    x = A_x
    xn = _layernorm(x, axis=-1)
    return x + _A_ref(xn, A_k_cache, A_v_cache, A_wq, A_wo) + _B_ref(xn, B_w_gate, B_w_up, B_w_down)
```

```cpp
#include <hip/hip_runtime.h>
#include <hip/hip_bf16.h>
#include <stdint.h>

typedef unsigned short u16;
typedef unsigned u32x4 __attribute__((ext_vector_type(4)));

constexpr int B = 2, D = 576, HQ = 32, HKV = 16, SK = 4096, DH = 128, F = 4608;
constexpr int NS = 8;

__device__ __forceinline__ float bf2f(u16 v) { return __uint_as_float((unsigned)v << 16); }
__device__ __forceinline__ float lo(unsigned v) { return __uint_as_float(v << 16); }
__device__ __forceinline__ float hi(unsigned v) { return __uint_as_float(v & 0xffff0000u); }
__device__ __forceinline__ float rbf(float x) { return (float)(__bf16)x; }

__device__ void layernorm(const u16* x, float* xn  , float* red) {
  int tid = threadIdx.x;
  for (int b = 0; b < B; ++b) {
    float s = 0.f;
    for (int i = tid; i < D; i += blockDim.x) s += bf2f(x[b * D + i]);
    red[tid] = s; __syncthreads();
    for (int o = 128; o > 0; o >>= 1) { if (tid < o) red[tid] += red[tid + o]; __syncthreads(); }
    float m = red[0] / D; __syncthreads();
    float v = 0.f;
    for (int i = tid; i < D; i += blockDim.x) { float d = bf2f(x[b * D + i]) - m; v += d * d; }
    red[tid] = v; __syncthreads();
    for (int o = 128; o > 0; o >>= 1) { if (tid < o) red[tid] += red[tid + o]; __syncthreads(); }
    float var = red[0] / D; __syncthreads();
    float rs = 1.f / sqrtf(var + 1e-5f);
    for (int i = tid; i < D; i += blockDim.x) xn[b * D + i] = rbf((bf2f(x[b * D + i]) - m) * rs);
    __syncthreads();
  }
}

__global__ void __launch_bounds__(256) k_proj(const u16* x, const u16* wq, const u16* wg, const u16* wu, float* q, float* h) {
  __shared__ float xn[B * D];
  __shared__ float red[256];
  __shared__ float acc[4][64][4];
  layernorm(x, xn, red);
  int blk = blockIdx.x, tid = threadIdx.x, w = tid >> 6, lane = tid & 63;
  if (blk < 64) {
    int n = blk * 64 + lane;
    float a0 = 0, a1 = 0;
    for (int k = w; k < D; k += 4) { float wv = bf2f(wq[(size_t)k * 4096 + n]); a0 += xn[k] * wv; a1 += xn[D + k] * wv; }
    acc[w][lane][0] = a0; acc[w][lane][1] = a1;
    __syncthreads();
    if (w == 0) {
      for (int b = 0; b < 2; ++b) q[b * 4096 + n] = rbf(acc[0][lane][b] + acc[1][lane][b] + acc[2][lane][b] + acc[3][lane][b]);
    }
  } else {
    int n = (blk - 64) * 64 + lane;
    float g0 = 0, g1 = 0, u0 = 0, u1 = 0;
    for (int k = w; k < D; k += 4) {
      float a = bf2f(wg[(size_t)k * F + n]), c = bf2f(wu[(size_t)k * F + n]);
      g0 += xn[k] * a; g1 += xn[D + k] * a; u0 += xn[k] * c; u1 += xn[D + k] * c;
    }
    acc[w][lane][0] = g0; acc[w][lane][1] = g1; acc[w][lane][2] = u0; acc[w][lane][3] = u1;
    __syncthreads();
    if (w == 0) {
      for (int b = 0; b < 2; ++b) {
        float g = acc[0][lane][b] + acc[1][lane][b] + acc[2][lane][b] + acc[3][lane][b];
        float u = acc[0][lane][2 + b] + acc[1][lane][2 + b] + acc[2][lane][2 + b] + acc[3][lane][2 + b];
        h[b * F + n] = g * u;
      }
    }
  }
}

__global__ void __launch_bounds__(256) k_attn(const u16* kc, const u16* vc, const float* q, float* po, float* pml) {
  __shared__ float sc[2][512];
  __shared__ float red[2][256];
  __shared__ float ored[16][2][128];
  int blk = blockIdx.x, s = blk & 7, kvh = (blk >> 3) & 15, b = blk >> 7;
  int tid = threadIdx.x, c = tid & 15, r = tid >> 4;
  const u16* K = kc + ((size_t)(b * HKV + kvh) * SK + s * 512) * DH;
  const u16* V = vc + ((size_t)(b * HKV + kvh) * SK + s * 512) * DH;
  float qv[2][8];
  for (int g = 0; g < 2; ++g) for (int j = 0; j < 8; ++j) qv[g][j] = q[b * 4096 + (kvh * 2 + g) * DH + 8 * c + j];
  for (int t = r; t < 512; t += 16) {
    u32x4 kk = *(const u32x4*)(K + (size_t)t * DH + 8 * c);
    float d0 = 0, d1 = 0;
    for (int j = 0; j < 4; ++j) {
      d0 += lo(kk[j]) * qv[0][2 * j] + hi(kk[j]) * qv[0][2 * j + 1];
      d1 += lo(kk[j]) * qv[1][2 * j] + hi(kk[j]) * qv[1][2 * j + 1];
    }
    for (int o = 8; o > 0; o >>= 1) { d0 += __shfl_xor(d0, o); d1 += __shfl_xor(d1, o); }
    if (c == 0) { sc[0][t] = d0 * 0.08838834764831845f; sc[1][t] = d1 * 0.08838834764831845f; }
  }
  __syncthreads();
  float m[2], l[2];
  for (int g = 0; g < 2; ++g) {
    red[g][tid] = fmaxf(sc[g][tid], sc[g][tid + 256]);
  }
  __syncthreads();
  for (int o = 128; o > 0; o >>= 1) { if (tid < o) { red[0][tid] = fmaxf(red[0][tid], red[0][tid + o]); red[1][tid] = fmaxf(red[1][tid], red[1][tid + o]); } __syncthreads(); }
  m[0] = red[0][0]; m[1] = red[1][0];
  __syncthreads();
  for (int g = 0; g < 2; ++g) {
    float p0 = __expf(sc[g][tid] - m[g]), p1 = __expf(sc[g][tid + 256] - m[g]);
    sc[g][tid] = p0; sc[g][tid + 256] = p1; red[g][tid] = p0 + p1;
  }
  __syncthreads();
  for (int o = 128; o > 0; o >>= 1) { if (tid < o) { red[0][tid] += red[0][tid + o]; red[1][tid] += red[1][tid + o]; } __syncthreads(); }
  l[0] = red[0][0]; l[1] = red[1][0];
  float a[2][8] = {};
  for (int t = r; t < 512; t += 16) {
    u32x4 vv = *(const u32x4*)(V + (size_t)t * DH + 8 * c);
    float p0 = sc[0][t], p1 = sc[1][t];
    for (int j = 0; j < 4; ++j) {
      a[0][2 * j] += p0 * lo(vv[j]); a[0][2 * j + 1] += p0 * hi(vv[j]);
      a[1][2 * j] += p1 * lo(vv[j]); a[1][2 * j + 1] += p1 * hi(vv[j]);
    }
  }
  for (int g = 0; g < 2; ++g) for (int j = 0; j < 8; ++j) ored[r][g][8 * c + j] = a[g][j];
  __syncthreads();
  {
    int g = tid >> 7, d = tid & 127;
    float o = 0;
    for (int i = 0; i < 16; ++i) o += ored[i][g][d];
    po[((size_t)(b * HQ + kvh * 2 + g) * NS + s) * DH + d] = o;
    if (d == 0) { pml[((b * HQ + kvh * 2 + g) * NS + s) * 2] = m[g]; pml[((b * HQ + kvh * 2 + g) * NS + s) * 2 + 1] = l[g]; }
  }
}

__global__ void __launch_bounds__(576) k_out(const u16* wo, const u16* wd, const float* po, const float* pml, const float* h, float* y) {
  __shared__ float a[2][128];
  int blk = blockIdx.x, tid = threadIdx.x;
  const u16* W;
  if (blk < 32) {
    W = wo + (size_t)blk * 128 * D;
    if (tid < 256) {
      int b = tid >> 7, d = tid & 127;
      const float* ml = pml + (b * HQ + blk) * NS * 2;
      float M = -INFINITY;
      for (int s = 0; s < NS; ++s) M = fmaxf(M, ml[2 * s]);
      float L = 0, o = 0;
      for (int s = 0; s < NS; ++s) { float w = __expf(ml[2 * s] - M); L += w * ml[2 * s + 1]; o += w * po[((size_t)(b * HQ + blk) * NS + s) * DH + d]; }
      a[b][d] = o / L;
    }
  } else {
    W = wd + (size_t)(blk - 32) * 128 * D;
    if (tid < 256) { int b = tid >> 7, d = tid & 127; a[b][d] = h[b * F + (blk - 32) * 128 + d]; }
  }
  __syncthreads();
  float y0 = 0, y1 = 0;
  for (int k = 0; k < 128; ++k) { float w = bf2f(W[(size_t)k * D + tid]); y0 += a[0][k] * w; y1 += a[1][k] * w; }
  y[(blk * 2 + 0) * D + tid] = y0; y[(blk * 2 + 1) * D + tid] = y1;
}

__global__ void __launch_bounds__(576) k_final(const u16* x, const float* y, __hip_bfloat16* out) {
  int b = blockIdx.x, tid = threadIdx.x;
  float s = 0;
  for (int i = 0; i < 68; ++i) s += y[(i * 2 + b) * D + tid];
  out[b * D + tid] = __float2bfloat16(bf2f(x[b * D + tid]) + s);
}

extern "C" void kernel_launch(void* const* d_in, const int* in_sizes, int n_in,
                              void* d_out, int out_size, void* d_ws, size_t ws_size,
                              hipStream_t stream) {
  const u16* x = (const u16*)d_in[0];
  const u16* kc = (const u16*)d_in[1];
  const u16* vc = (const u16*)d_in[2];
  const u16* wq = (const u16*)d_in[3];
  const u16* wo = (const u16*)d_in[4];
  const u16* wg = (const u16*)d_in[6];
  const u16* wu = (const u16*)d_in[7];
  const u16* wd = (const u16*)d_in[8];
  float* ws = (float*)d_ws;
  float* q = ws;
  float* h = q + 8192;
  float* po = h + 9216;
  float* pml = po + 65536;
  float* y = pml + 1024;
  k_proj<<<64 + 72, 256, 0, stream>>>(x, wq, wg, wu, q, h);
  k_attn<<<256, 256, 0, stream>>>(kc, vc, q, po, pml);
  k_out<<<68, 576, 0, stream>>>(wo, wd, po, pml, h, y);
  k_final<<<2, 576, 0, stream>>>(x, y, (__hip_bfloat16*)d_out);
}
```

```cpp
#include <hip/hip_runtime.h>
#include <hip/hip_bf16.h>
#include <stdint.h>

typedef unsigned short u16;
typedef unsigned u32x4 __attribute__((ext_vector_type(4)));

constexpr int B = 2, D = 576, HQ = 32, HKV = 16, SK = 4096, DH = 128, F = 4608;
constexpr int NS = 8;

__device__ __forceinline__ float bf2f(u16 v) { return __uint_as_float((unsigned)v << 16); }
__device__ __forceinline__ float lo(unsigned v) { return __uint_as_float(v << 16); }
__device__ __forceinline__ float hi(unsigned v) { return __uint_as_float(v & 0xffff0000u); }
__device__ __forceinline__ float rbf(float x) { return (float)(__bf16)x; }

__device__ void layernorm(const u16* x, float* xn  , float* red) {
  int tid = threadIdx.x;
  for (int b = 0; b < B; ++b) {
    float s = 0.f;
    for (int i = tid; i < D; i += blockDim.x) s += bf2f(x[b * D + i]);
    red[tid] = s; __syncthreads();
    for (int o = 128; o > 0; o >>= 1) { if (tid < o) red[tid] += red[tid + o]; __syncthreads(); }
    float m = red[0] / D; __syncthreads();
    float v = 0.f;
    for (int i = tid; i < D; i += blockDim.x) { float d = bf2f(x[b * D + i]) - m; v += d * d; }
    red[tid] = v; __syncthreads();
    for (int o = 128; o > 0; o >>= 1) { if (tid < o) red[tid] += red[tid + o]; __syncthreads(); }
    float var = red[0] / D; __syncthreads();
    float sd = rbf(sqrtf(rbf(rbf(var) + 1e-5f)));
    float mb = rbf(m);
    for (int i = tid; i < D; i += blockDim.x) xn[b * D + i] = rbf(rbf(bf2f(x[b * D + i]) - mb) / sd);
    __syncthreads();
  }
}

__global__ void __launch_bounds__(256) k_proj(const u16* x, const u16* wq, const u16* wg, const u16* wu, float* q, float* h) {
  __shared__ float xn[B * D];
  __shared__ float red[256];
  __shared__ float acc[4][64][4];
  layernorm(x, xn, red);
  int blk = blockIdx.x, tid = threadIdx.x, w = tid >> 6, lane = tid & 63;
  if (blk < 64) {
    int n = blk * 64 + lane;
    float a0 = 0, a1 = 0;
    for (int k = w; k < D; k += 4) { float wv = bf2f(wq[(size_t)k * 4096 + n]); a0 += xn[k] * wv; a1 += xn[D + k] * wv; }
    acc[w][lane][0] = a0; acc[w][lane][1] = a1;
    __syncthreads();
    if (w == 0) {
      for (int b = 0; b < 2; ++b) q[b * 4096 + n] = rbf(acc[0][lane][b] + acc[1][lane][b] + acc[2][lane][b] + acc[3][lane][b]);
    }
  } else {
    int n = (blk - 64) * 64 + lane;
    float g0 = 0, g1 = 0, u0 = 0, u1 = 0;
    for (int k = w; k < D; k += 4) {
      float a = bf2f(wg[(size_t)k * F + n]), c = bf2f(wu[(size_t)k * F + n]);
      g0 += xn[k] * a; g1 += xn[D + k] * a; u0 += xn[k] * c; u1 += xn[D + k] * c;
    }
    acc[w][lane][0] = g0; acc[w][lane][1] = g1; acc[w][lane][2] = u0; acc[w][lane][3] = u1;
    __syncthreads();
    if (w == 0) {
      for (int b = 0; b < 2; ++b) {
        float g = acc[0][lane][b] + acc[1][lane][b] + acc[2][lane][b] + acc[3][lane][b];
        float u = acc[0][lane][2 + b] + acc[1][lane][2 + b] + acc[2][lane][2 + b] + acc[3][lane][2 + b];
        h[b * F + n] = g * u;
      }
    }
  }
}

#define KVAUX 2
__global__ void __launch_bounds__(1024) k_attn(const u16* kc, const u16* vc, const float* q, float* po, float* pml) {
  __shared__ float sc[2][512];
  __shared__ float red[2][16];
  __shared__ float ored[16][2][128];
  int blk = blockIdx.x, s = blk & 7, kvh = (blk >> 3) & 15, b = blk >> 7;
  int tid = threadIdx.x, c = tid & 15, r = tid >> 4, w = tid >> 6, lane = tid & 63;
  const u16* K = kc + ((size_t)(b * HKV + kvh) * SK + s * 512) * DH;
  const u16* V = vc + ((size_t)(b * HKV + kvh) * SK + s * 512) * DH;
  __amdgpu_buffer_rsrc_t rk = __builtin_amdgcn_make_buffer_rsrc((void*)K, 0, 512 * 256, 0x00020000);
  __amdgpu_buffer_rsrc_t rv = __builtin_amdgcn_make_buffer_rsrc((void*)V, 0, 512 * 256, 0x00020000);
  float qv[2][8];
  for (int g = 0; g < 2; ++g) for (int j = 0; j < 8; ++j) qv[g][j] = q[b * 4096 + (kvh * 2 + g) * DH + 8 * c + j];
  u32x4 kk[8], vv[8];
#pragma unroll
  for (int i = 0; i < 8; ++i) kk[i] = __builtin_amdgcn_raw_buffer_load_b128(rk, tid * 16 + i * 16384, 0, KVAUX);
#pragma unroll
  for (int i = 0; i < 8; ++i) vv[i] = __builtin_amdgcn_raw_buffer_load_b128(rv, tid * 16 + i * 16384, 0, KVAUX);
  __builtin_amdgcn_sched_barrier(0);
#pragma unroll
  for (int i = 0; i < 8; ++i) {
    int t = r + 64 * i;
    float d0 = 0, d1 = 0;
    for (int j = 0; j < 4; ++j) {
      d0 += lo(kk[i][j]) * qv[0][2 * j] + hi(kk[i][j]) * qv[0][2 * j + 1];
      d1 += lo(kk[i][j]) * qv[1][2 * j] + hi(kk[i][j]) * qv[1][2 * j + 1];
    }
    for (int o = 8; o > 0; o >>= 1) { d0 += __shfl_xor(d0, o); d1 += __shfl_xor(d1, o); }
    if (c == 0) { sc[0][t] = rbf(rbf(d0) * 0.08838834764831845f); sc[1][t] = rbf(rbf(d1) * 0.08838834764831845f); }
  }
  __syncthreads();
  float m[2], l[2];
  {
    int g = tid >> 9, t = tid & 511;
    float v = sc[g][t], mx = v;
    for (int o = 32; o > 0; o >>= 1) mx = fmaxf(mx, __shfl_xor(mx, o));
    if (lane == 0) red[0][w] = mx;
    __syncthreads();
    for (int gg = 0; gg < 2; ++gg) { float a = red[0][gg * 8]; for (int i = 1; i < 8; ++i) a = fmaxf(a, red[0][gg * 8 + i]); m[gg] = a; }
    float p = __expf(v - m[g]), sm = p;
    sc[g][t] = p;
    for (int o = 32; o > 0; o >>= 1) sm += __shfl_xor(sm, o);
    if (lane == 0) red[1][w] = sm;
    __syncthreads();
    for (int gg = 0; gg < 2; ++gg) { float a = red[1][gg * 8]; for (int i = 1; i < 8; ++i) a += red[1][gg * 8 + i]; l[gg] = a; }
  }
  float a[2][8] = {};
#pragma unroll
  for (int i = 0; i < 8; ++i) {
    int t = r + 64 * i;
    float p0 = sc[0][t], p1 = sc[1][t];
    for (int j = 0; j < 4; ++j) {
      a[0][2 * j] += p0 * lo(vv[i][j]); a[0][2 * j + 1] += p0 * hi(vv[i][j]);
      a[1][2 * j] += p1 * lo(vv[i][j]); a[1][2 * j + 1] += p1 * hi(vv[i][j]);
    }
  }
  for (int g = 0; g < 2; ++g) for (int j = 0; j < 8; ++j) { float t = a[g][j]; t += __shfl_xor(t, 16); t += __shfl_xor(t, 32); if (lane < 16) ored[w][g][8 * c + j] = t; }
  __syncthreads();
  if (tid < 256) {
    int g = tid >> 7, d = tid & 127;
    float o = 0;
    for (int i = 0; i < 16; ++i) o += ored[i][g][d];
    po[((size_t)(b * HQ + kvh * 2 + g) * NS + s) * DH + d] = o;
    if (d == 0) { pml[((b * HQ + kvh * 2 + g) * NS + s) * 2] = m[g]; pml[((b * HQ + kvh * 2 + g) * NS + s) * 2 + 1] = l[g]; }
  }
}

__global__ void __launch_bounds__(576) k_out(const u16* wo, const u16* wd, const float* po, const float* pml, const float* h, float* y) {
  __shared__ float a[2][128];
  int blk = blockIdx.x, tid = threadIdx.x;
  const u16* W;
  if (blk < 32) {
    W = wo + (size_t)blk * 128 * D;
    if (tid < 256) {
      int b = tid >> 7, d = tid & 127;
      const float* ml = pml + (b * HQ + blk) * NS * 2;
      float M = -INFINITY;
      for (int s = 0; s < NS; ++s) M = fmaxf(M, ml[2 * s]);
      float L = 0, o = 0;
      for (int s = 0; s < NS; ++s) { float w = __expf(ml[2 * s] - M); L += w * ml[2 * s + 1]; o += w * po[((size_t)(b * HQ + blk) * NS + s) * DH + d]; }
      a[b][d] = o / L;
    }
  } else {
    W = wd + (size_t)(blk - 32) * 128 * D;
    if (tid < 256) { int b = tid >> 7, d = tid & 127; a[b][d] = h[b * F + (blk - 32) * 128 + d]; }
  }
  __syncthreads();
  float y0 = 0, y1 = 0;
  for (int k = 0; k < 128; ++k) { float w = bf2f(W[(size_t)k * D + tid]); y0 += a[0][k] * w; y1 += a[1][k] * w; }
  y[(blk * 2 + 0) * D + tid] = y0; y[(blk * 2 + 1) * D + tid] = y1;
}

__global__ void __launch_bounds__(576) k_final(const u16* x, const float* y, __hip_bfloat16* out) {
  int b = blockIdx.x, tid = threadIdx.x;
  float s = 0;
  for (int i = 0; i < 68; ++i) s += y[(i * 2 + b) * D + tid];
  out[b * D + tid] = __float2bfloat16(bf2f(x[b * D + tid]) + s);
}

extern "C" void kernel_launch(void* const* d_in, const int* in_sizes, int n_in,
                              void* d_out, int out_size, void* d_ws, size_t ws_size,
                              hipStream_t stream) {
  const u16* x = (const u16*)d_in[0];
  const u16* kc = (const u16*)d_in[1];
  const u16* vc = (const u16*)d_in[2];
  const u16* wq = (const u16*)d_in[3];
  const u16* wo = (const u16*)d_in[4];
  const u16* wg = (const u16*)d_in[6];
  const u16* wu = (const u16*)d_in[7];
  const u16* wd = (const u16*)d_in[8];
  float* ws = (float*)d_ws;
  float* q = ws;
  float* h = q + 8192;
  float* po = h + 9216;
  float* pml = po + 65536;
  float* y = pml + 1024;
  k_proj<<<64 + 72, 256, 0, stream>>>(x, wq, wg, wu, q, h);
  k_attn<<<256, 1024, 0, stream>>>(kc, vc, q, po, pml);
  k_out<<<68, 576, 0, stream>>>(wo, wd, po, pml, h, y);
  k_final<<<2, 576, 0, stream>>>(x, y, (__hip_bfloat16*)d_out);
}
```

```cpp
#include <hip/hip_runtime.h>
#include <hip/hip_bf16.h>
#include <stdint.h>

namespace {

typedef unsigned short u16;
typedef unsigned long long u64;
typedef unsigned u32x2 __attribute__((ext_vector_type(2)));
typedef unsigned u32x3 __attribute__((ext_vector_type(3)));
typedef unsigned u32x4 __attribute__((ext_vector_type(4)));
typedef float f32x2 __attribute__((ext_vector_type(2)));
typedef float f32x4 __attribute__((ext_vector_type(4)));
typedef __bf16 bf16x2 __attribute__((ext_vector_type(2)));
typedef __bf16 bf16x8 __attribute__((ext_vector_type(8)));
typedef __amdgpu_buffer_rsrc_t rsrc_t;
#define LAS __attribute__((address_space(3)))

constexpr int D = 576, SK = 4096, DH = 128, F = 4608, NQ = 4096;
constexpr int NWG = 256, NTHR = 512;
constexpr int AUX_NT = 2, AUX_SC1 = 16;
constexpr int OOB = 0x7ffffff0;
constexpr unsigned EMPTY = 0xFFFFFFFFu;
constexpr unsigned SPIN_CAP = 1u << 16;
#define WIN 8
#define FIN_DELAY 4

constexpr int WS_E = 0;
constexpr int WS_FLAG = 4096;
constexpr int WS_ST = 8192;
constexpr int WS_Q = 16384;
constexpr int WS_O = WS_Q + 65536;
constexpr int WS_GU = WS_O + 262144;
constexpr int WS_Y = WS_GU + 192 * 3072;
constexpr int WS_END = WS_Y + 72 * 16384;

constexpr int L_CNT = 0;
enum { C_INIT = 0, C_QP, F_Q, C_ST, C_D, F_OK, F_H, F_O, F_V, C_FIN, N_CNT = 16 };
constexpr int L_Q = 16;
constexpr int L_ST = L_Q + 128;
constexpr int L_CH = L_ST + 16;
constexpr int L_CO = L_CH + 48;
constexpr int L_XR = L_CO + 32;
constexpr int L_GU = L_XR + 8;
constexpr int L_FIN = L_GU + 96;
constexpr int L_D = L_FIN + 128;
constexpr int L_QP = L_D + 1024;
constexpr int L_END = L_QP + 1024;

struct Args { const u16 *x, *k, *v, *wq, *wo, *wg, *wu, *wd; u16* out; unsigned char* ws; };

template <int I> struct IC { static constexpr int value = I; };
template <int N, int I = 0, class Fn> __device__ __forceinline__ void static_for(Fn&& f) {
  if constexpr (I < N) { f(IC<I>{}); static_for<N, I + 1>(f); }
}
__device__ __forceinline__ rsrc_t mk(const void* p, int bytes) { return __builtin_amdgcn_make_buffer_rsrc((void*)p, 0, bytes, 0x00020000); }
__device__ __forceinline__ u32x4 ld_nt(rsrc_t r, int voff, int soff) { return __builtin_amdgcn_raw_buffer_load_b128(r, voff, soff, AUX_NT); }
__device__ __forceinline__ u32x4 ld_sc(rsrc_t r, int voff, int soff) { return __builtin_amdgcn_raw_buffer_load_b128(r, voff, soff, AUX_SC1); }
__device__ __forceinline__ void st_sc(rsrc_t r, int voff, int soff, u32x4 v) { __builtin_amdgcn_raw_buffer_store_b128(v, r, voff, soff, AUX_SC1); }
__device__ __forceinline__ float asf(unsigned u) { return __builtin_bit_cast(float, u); }
__device__ __forceinline__ unsigned asu(float f) { return __builtin_bit_cast(unsigned, f); }
__device__ __forceinline__ float lo(unsigned v) { return asf(v << 16); }
__device__ __forceinline__ float hi(unsigned v) { return asf(v & 0xffff0000u); }
__device__ __forceinline__ unsigned pk(float a, float b) { const f32x2 v = {a, b}; return __builtin_bit_cast(unsigned, __builtin_convertvector(v, bf16x2)); }
__device__ __forceinline__ float rbf(float a) { return lo(pk(a, a)); }
__device__ __forceinline__ float dot2(unsigned a, unsigned b, float c) {
  return __builtin_amdgcn_fdot2_f32_bf16(__builtin_bit_cast(bf16x2, a), __builtin_bit_cast(bf16x2, b), c, false);
}
template <int CTRL> __device__ __forceinline__ float dpp(float x) {
  return asf(__builtin_amdgcn_update_dpp(0, (int)asu(x), CTRL, 0xf, 0xf, true));
}
constexpr int XOR1 = 0xB1, XOR2 = 0x4E, HMIR = 0x141, MIR = 0x140, ROR4 = 0x124, ROR8 = 0x128;
__device__ __forceinline__ float sum8(float a) { a += dpp<XOR1>(a); a += dpp<XOR2>(a); return a + dpp<HMIR>(a); }
__device__ __forceinline__ float max8(float a) { a = fmaxf(a, dpp<XOR1>(a)); a = fmaxf(a, dpp<XOR2>(a)); return fmaxf(a, dpp<HMIR>(a)); }
__device__ __forceinline__ float sum_x16(float a) { auto s = __builtin_amdgcn_permlane16_swap(asu(a), asu(a), false, false); return asf(s[0]) + asf(s[1]); }
__device__ __forceinline__ float sum_x32(float a) { auto s = __builtin_amdgcn_permlane32_swap(asu(a), asu(a), false, false); return asf(s[0]) + asf(s[1]); }
__device__ __forceinline__ float max_x16(float a) { auto s = __builtin_amdgcn_permlane16_swap(asu(a), asu(a), false, false); return fmaxf(asf(s[0]), asf(s[1])); }
__device__ __forceinline__ float max_x32(float a) { auto s = __builtin_amdgcn_permlane32_swap(asu(a), asu(a), false, false); return fmaxf(asf(s[0]), asf(s[1])); }
__device__ __forceinline__ f32x4 gemv4(unsigned coef2, const u32x4& mask, const u32x4& w, f32x4 acc) {
  const u32x4 a = {coef2 & mask.x, coef2 & mask.y, coef2 & mask.z, coef2 & mask.w};
  return __builtin_amdgcn_mfma_f32_16x16x32_bf16(__builtin_bit_cast(bf16x8, a), __builtin_bit_cast(bf16x8, w), acc, 0, 0, 0);
}
__device__ __forceinline__ u32x4 payload(f32x4 v) {
  return u32x4{min(asu(v.x), EMPTY - 1), min(asu(v.y), EMPTY - 1), min(asu(v.z), EMPTY - 1), min(asu(v.w), EMPTY - 1)};
}
__device__ __forceinline__ bool filled(const u32x4& v) { return max(max(v.x, v.y), max(v.z, v.w)) != EMPTY; }
#define CBAR() asm volatile("" ::: "memory")
#define DRAIN_VM() asm volatile("s_waitcnt vmcnt(0)" ::: "memory")
template <int N> __device__ __forceinline__ void throttle() { asm volatile("s_waitcnt vmcnt(%0)" ::"i"(N) : "memory"); }
__device__ __forceinline__ void lds_signal(LAS unsigned* p) {
  __builtin_amdgcn_fence(__ATOMIC_RELEASE, "workgroup", "local");
  if ((threadIdx.x & 63) == 0) __hip_atomic_fetch_add(p, 1u, __ATOMIC_RELAXED, __HIP_MEMORY_SCOPE_WORKGROUP);
  CBAR();
}
__device__ __forceinline__ void lds_wait(LAS unsigned* p, unsigned n) {
  while ((unsigned)__builtin_amdgcn_readfirstlane(__hip_atomic_load(p, __ATOMIC_RELAXED, __HIP_MEMORY_SCOPE_WORKGROUP)) < n) __builtin_amdgcn_s_sleep(1);
  __builtin_amdgcn_fence(__ATOMIC_ACQUIRE, "workgroup", "local");
}
#define TS(i)
#define TSDECL
#define TSFLUSH()

__device__ __forceinline__ void normalize18(const u32x3 (&r)[3], float m, float sd, float rs, unsigned (&o)[18]) {
#pragma unroll
  for (int i = 0; i < 9; ++i) {
    const unsigned t = r[i / 3][i % 3];
    const unsigned d = pk(lo(t) - m, hi(t) - m);
    const float d0 = lo(d), d1 = hi(d);
    float q0 = d0 * rs, q1 = d1 * rs;
    q0 = fmaf(fmaf(-q0, sd, d0), rs, q0);
    q1 = fmaf(fmaf(-q1, sd, d1), rs, q1);
    o[2 * i] = pk(q0, q0);
    o[2 * i + 1] = pk(q1, q1);
  }
}

__global__ void __launch_bounds__(NTHR, 2) fused_decode(Args a) {
  __shared__ __attribute__((aligned(16))) unsigned lds_raw[L_END];
  LAS unsigned* L = (LAS unsigned*)lds_raw;
  TSDECL;
  TS(0);
  const int lane = threadIdx.x & 63, w = __builtin_amdgcn_readfirstlane(threadIdx.x >> 6);
  const int c = blockIdx.x, s = c & 7, pair = c >> 3, bb = pair & 1, kvh = pair >> 1;
  const int sr = (s + pair) & 7;
  const bool isq = (sr & 3) == 0;
  const int qi = pair * 2 + (sr >> 2), gi = pair * 6 + (sr >> 2) * 3 + (sr & 3) - 1;
  const int G = lane >> 4, cc = lane & 15, sel = (lane >> 3) & 1;
  const rsrc_t rw = mk(a.ws, WS_END);
  const u32x4 mask = {(lane & 7) == 0 ? 0xffffu : (lane & 7) == 1 ? 0xffff0000u : 0u, (lane & 7) == 2 ? 0xffffu : (lane & 7) == 3 ? 0xffff0000u : 0u,
                      (lane & 7) == 4 ? 0xffffu : (lane & 7) == 5 ? 0xffff0000u : 0u, (lane & 7) == 6 ? 0xffffu : (lane & 7) == 7 ? 0xffff0000u : 0u};
  const u32x4 empty4 = {EMPTY, EMPTY, EMPTY, EMPTY};
  const int j = bb * 8 + s;
  const int seg = w < 4 ? w : 4;
  const bool segok = seg < 4 || cc < 8;
  const int yoff = segok ? WS_Y + ((seg * 16 + (seg < 4 ? (lane >> 2) : G * 2 + (cc >> 2))) * 256 + c) * 64 + (lane & 3) * 16 : OOB;
  const int wo_off = segok ? (kvh * 256 + (j >> 3) * 128 + 8 * (4 * (j & 3) + G) + 4 * ((j >> 2) & 1)) * (D * 2) + seg * 256 + cc * 16 : OOB;
  const int wd_off = segok && !isq ? (gi * 24 + 6 * G) * (D * 2) + seg * 256 + cc * 16 : OOB;
  const rsrc_t ro = mk(a.wo, NQ * D * 2), rd = mk(a.wd, F * D * 2);
  u32x4 W3[10];
  f32x4 y = {0.f, 0.f, 0.f, 0.f};

  if (w < 4) {
    if (w == 0 && lane < N_CNT) L[L_CNT + lane] = 0u;
    st_sc(rw, yoff, 0, empty4);
    const size_t kvbase = ((size_t)(bb * 16 + kvh) * SK + s * 512) * DH;
    const rsrc_t rk = mk(a.k + kvbase, 512 * DH * 2), rv = mk(a.v + kvbase, 512 * DH * 2);
    const int voff = w * 1024 + lane * 16;
    asm volatile("s_waitcnt lgkmcnt(0)" ::: "memory");
    __builtin_amdgcn_s_barrier();
    CBAR();
    TS(1);
    u32x4 K[32], V[32], qa, qb;
    float sc[32];
    f32x4 acc = {0.f, 0.f, 0.f, 0.f};
    float mx = -INFINITY, lsum = 0.f, mneg = 0.f;
    constexpr int QK0 = 32, PV0 = QK0 + 16;
    static_for<74>([&](auto ic) {
      constexpr int n = ic.value;
      if constexpr (n >= WIN) throttle<WIN - 1>();
      if constexpr (n < 32) K[n] = ld_nt(rk, voff, n * 4096);
      else if constexpr (n < 64) V[n - 32] = ld_nt(rv, voff, (n - 32) * 4096);
      else if constexpr (n < 68) W3[n - 64] = ld_nt(ro, wo_off, (n - 64) * (D * 2));
      else W3[n - 64] = ld_nt(rd, wd_off, (n - 68) * (D * 2));
      if constexpr (n == WIN) lds_signal(L + C_INIT);
      if constexpr (n == 60) { if (w == 0) lds_signal(L + F_V); }
      if constexpr (n == QK0) {
        TS(2);
        lds_wait(L + F_Q, 1);
        qa = *(LAS u32x4*)(L + L_Q + sel * 64 + cc * 4);
        qb = *(LAS u32x4*)(L + L_Q + (sel ^ 1) * 64 + cc * 4);
        TS(3);
      }
      if constexpr (n >= QK0 && n < PV0) {
        constexpr int i = 2 * (n - QK0);
        float t[2];
#pragma unroll
        for (int e = 0; e < 2; ++e) {
          float u = 0.f, v = 0.f;
#pragma unroll
          for (int d = 0; d < 4; ++d) { const unsigned kk = K[i + e][d], q0 = qa[d], q1 = qb[d]; u = dot2(kk, q0, u); v = dot2(kk, q1, v); }
          t[e] = sum8(u + dpp<ROR8>(v));
        }
        const unsigned r1 = pk(t[0], t[1]);
        const unsigned r2 = pk(lo(r1) * 0.08838834764831845f, hi(r1) * 0.08838834764831845f);
        sc[i] = lo(r2); sc[i + 1] = hi(r2);
        mx = fmaxf(mx, fmaxf(sc[i], sc[i + 1]));
      }
      if constexpr (n == PV0 - 1) {
        mx = max_x32(max_x16(mx));
        mneg = -mx * 1.4426950408889634f;
#pragma unroll
        for (int i = 0; i < 32; ++i) { sc[i] = __builtin_amdgcn_exp2f(fmaf(sc[i], 1.4426950408889634f, mneg)); lsum += sc[i]; }
        lsum = sum_x32(sum_x16(lsum));
        if ((lane & 0x37) == 0) *(LAS f32x2*)(L + L_ST + w * 4 + sel * 2) = f32x2{mx, lsum};
        lds_signal(L + C_ST);
        TS(4);
      }
      if constexpr (n >= PV0) {
        constexpr int done = (n - 1 - PV0) * 2 + 2 < n - 1 - 32 - WIN + 1 ? (n - 1 - PV0) * 2 + 2 : n - 1 - 32 - WIN + 1;
        constexpr int upto = (n - PV0) * 2 + 2 < n - 32 - WIN + 1 ? (n - PV0) * 2 + 2 : n - 32 - WIN + 1;
        static_for<32>([&](auto jc) {
          constexpr int i = jc.value;
          if constexpr (i >= (n == PV0 ? 0 : done) && i < upto) acc = gemv4(pk(sc[i], sc[i]), mask, V[i], acc);
          if constexpr (i == 31 && i >= done && i < upto) {
            TS(5);
            *(LAS f32x4*)(L + L_D + w * 256 + lane * 4) = acc;
            lds_signal(L + C_D);
          }
        });
      }
      __builtin_amdgcn_sched_barrier(0);
    });
    static_assert(73 - 32 - WIN + 1 >= 32, "all V loads are consumed inside the step list");
  } else {
    __builtin_amdgcn_s_setprio(2);
    const int ww = w - 4;
    const rsrc_t rx = mk(a.x, 2 * D * 2);
    const u32x2 ev = __builtin_amdgcn_raw_buffer_load_b64(rw, WS_E, 0, AUX_SC1);
    u32x3 xs[3], x0[3], x1[3];
#pragma unroll
    for (int i = 0; i < 3; ++i) xs[i] = __builtin_amdgcn_raw_buffer_load_b96(rx, lane * 36 + i * 12, 0, 0);
    const int sl = isq ? ww : gi * 3 + ww;
    const bool act = isq || ww < 3;
    const int r0 = isq ? 144 * ww + 18 * (2 * G + sel) : 72 * (sl & 7) + 18 * G;
    const int blk = sl >> 3, mat = blk >= 36, cb = blk - 36 * mat;
#pragma unroll
    for (int i = 0; i < 3; ++i) {
      x0[i] = __builtin_amdgcn_raw_buffer_load_b96(rx, (isq ? 0 : sel * (D * 2)) + r0 * 2 + i * 12, 0, 0);
      x1[i] = __builtin_amdgcn_raw_buffer_load_b96(rx, isq ? D * 2 + r0 * 2 + i * 12 : OOB, 0, 0);
    }
    const int rstride = isq ? NQ * 2 : F * 2;
    const rsrc_t r1 = mk(isq ? a.wq : mat ? a.wu : a.wg, D * rstride);
    const int woff = !act ? OOB : isq ? r0 * rstride + qi * 128 + (lane & 7) * 16 : r0 * rstride + cb * 256 + cc * 16;
    u32x4 Wt[18];
#pragma unroll
    for (int i = 0; i < 18; ++i) Wt[i] = ld_nt(r1, woff, i * rstride);
    __builtin_amdgcn_sched_barrier(0);
    __builtin_amdgcn_s_barrier();
    CBAR();
    TS(1);
    const int ooff = WS_O + ((kvh * 16 + (lane >> 2)) * 16 + j) * 64 + (lane & 3) * 16;
    const int stoff = (lane & 31) == 0 ? WS_ST + ((kvh * 2 + (lane >> 5)) * 16 + j) * 8 : OOB;
    if (ww == 0) {
      st_sc(rw, ooff, 0, empty4);
      __builtin_amdgcn_raw_buffer_store_b64(u32x2{EMPTY, EMPTY}, rw, stoff, 0, AUX_SC1);
    } else if (ww == 1) st_sc(rw, yoff, 0, empty4);
    float sm = 0.f;
#pragma unroll
    for (int i = 0; i < 9; ++i) { const unsigned t = xs[i / 3][i % 3]; sm += lo(t) + hi(t); }
    sm = sum8(sm); sm += dpp<MIR>(sm); sm = sum_x16(sm);
    const float mean = sm * (1.f / D);
    float sv = 0.f;
#pragma unroll
    for (int i = 0; i < 9; ++i) { const unsigned t = xs[i / 3][i % 3]; const float d0 = lo(t) - mean, d1 = hi(t) - mean; sv = fmaf(d0, d0, fmaf(d1, d1, sv)); }
    sv = sum8(sv); sv += dpp<MIR>(sv); sv = sum_x16(sv);
    const float mb = rbf(mean), sd = rbf(sqrtf(rbf(rbf(sv * (1.f / D)) + 1e-5f))), rs = __builtin_amdgcn_rcpf(sd);
    const float m0 = asf(__builtin_amdgcn_readlane(asu(mb), 0)), m1 = asf(__builtin_amdgcn_readlane(asu(mb), 32));
    const float s0 = asf(__builtin_amdgcn_readlane(asu(sd), 0)), s1 = asf(__builtin_amdgcn_readlane(asu(sd), 32));
    const float i0 = asf(__builtin_amdgcn_readlane(asu(rs), 0)), i1 = asf(__builtin_amdgcn_readlane(asu(rs), 32));
    const u64 tag = (((u64)(unsigned)__builtin_amdgcn_readfirstlane(ev.y) << 32) | (unsigned)__builtin_amdgcn_readfirstlane(ev.x)) + 1;
    const unsigned tag_lo16 = (unsigned)tag << 16, tag_hi = (unsigned)(tag >> 16);
    if (isq) {
      unsigned c0[18], c1[18];
      normalize18(x0, m0, s0, i0, c0);
      normalize18(x1, m1, s1, i1, c1);
      f32x4 d0 = {0.f, 0.f, 0.f, 0.f}, d1 = d0;
#pragma unroll
      for (int i = 0; i < 18; ++i) { d0 = gemv4(c0[i], mask, Wt[i], d0); d1 = gemv4(c1[i], mask, Wt[i], d1); }
      TS(2);
      if (sel == (lane >> 5)) {
        LAS f32x4* qp = (LAS f32x4*)(L + L_QP) + ww * 64 + sel * 16 + (G & 1) * 8 + (lane & 7);
        qp[0] = d0; qp[32] = d1;
      }
      lds_signal(L + C_QP);
      if (ww == 0) {
        lds_wait(L + C_QP, 4);
        const int rr = lane >> 5, b = G & 1;
        f32x2 t = {0.f, 0.f};
#pragma unroll
        for (int i = 0; i < 8; ++i) t += *(LAS f32x2*)(L + L_QP + ((i >> 1) * 64 + b * 32 + (i & 1) * 16 + cc) * 4 + rr * 2);
        const unsigned qq = pk(t.x, t.y);
        st_sc(rw, WS_Q + (((qi >> 2) * 2 + b) * 256 + (qi & 3) * 64 + (lane & 7) * 8 + sel * 4 + rr * 2) * 8, 0,
              u32x4{(qq & 0xffffu) | tag_lo16, tag_hi, (qq >> 16) | tag_lo16, tag_hi});
      }
    } else if (act) {
      unsigned c0[18];
      normalize18(x0, sel ? m1 : m0, sel ? s1 : s0, sel ? i1 : i0, c0);
      f32x4 d0 = {0.f, 0.f, 0.f, 0.f};
#pragma unroll
      for (int i = 0; i < 18; ++i) d0 = gemv4(c0[i], mask, Wt[i], d0);
      TS(2);
      const int f4 = cb * 32 + cc * 2 + (G & 1), cons = f4 / 6;
      st_sc(rw, WS_GU + cons * 3072 + ((mat * 12 + (G >> 1) * 6 + (f4 - cons * 6)) * 8 + (sl & 7)) * 16, 0, __builtin_bit_cast(u32x4, d0));
    }
    if (ww == 3 && c < 72 && lane < 4) {
      const int sg = c >> 4, g2 = sg < 4 ? (c >> 2) & 3 : (c >> 1) & 3, mm = sg < 4 ? c & 3 : c & 1;
      *(LAS u32x2*)(L + L_XR + lane * 2) = __builtin_amdgcn_raw_buffer_load_b64(rx, ((g2 >> 1) * D + 128 * sg + 8 * (4 * mm + lane) + 4 * (g2 & 1)) * 2, 0, 0);
    }
    DRAIN_VM();
    TS(3);
    if (ww != 1) lds_signal(L + C_INIT);

    if (ww == 0) {
      u32x4 g0, g1;
      const int qoff = WS_Q + (kvh * 2 + bb) * 2048 + lane * 16;
      for (unsigned spin = 0; spin < SPIN_CAP; ++spin) {
        CBAR();
        g0 = ld_sc(rw, qoff, 0); g1 = ld_sc(rw, qoff, 1024);
        const bool ok = ((g0.x & 0xffff0000u) == tag_lo16) & (g0.y == tag_hi) & ((g0.z & 0xffff0000u) == tag_lo16) & (g0.w == tag_hi) &
                        ((g1.x & 0xffff0000u) == tag_lo16) & (g1.y == tag_hi) & ((g1.z & 0xffff0000u) == tag_lo16) & (g1.w == tag_hi);
        if (__all(ok)) break;
        __builtin_amdgcn_s_sleep(8);
      }
      L[L_Q + lane] = (g0.x & 0xffffu) | (g0.z << 16);
      L[L_Q + 64 + lane] = (g1.x & 0xffffu) | (g1.z << 16);
      lds_signal(L + F_Q);
      TS(4);
      lds_wait(L + C_ST, 4);
      f32x2 st[4];
#pragma unroll
      for (int i = 0; i < 4; ++i) st[i] = *(LAS f32x2*)(L + L_ST + i * 4 + (lane >> 5) * 2);
      const float M = fmaxf(fmaxf(st[0].x, st[1].x), fmaxf(st[2].x, st[3].x));
      float fw[4], Ls = 0.f;
#pragma unroll
      for (int i = 0; i < 4; ++i) { fw[i] = __builtin_amdgcn_exp2f((st[i].x - M) * 1.4426950408889634f); Ls = fmaf(fw[i], st[i].y, Ls); }
      __builtin_amdgcn_raw_buffer_store_b64(u32x2{min(asu(M), EMPTY - 1), min(asu(Ls), EMPTY - 1)}, rw, stoff, 0, AUX_SC1);
      lds_wait(L + F_OK, 1);
      TS(5);
      lds_wait(L + C_D, 4);
      f32x4 o = {0.f, 0.f, 0.f, 0.f};
#pragma unroll
      for (int i = 0; i < 4; ++i) o += fw[i] * *(LAS f32x4*)(L + L_D + i * 256 + lane * 4);
      st_sc(rw, ooff, 0, payload(o));
      TS(6);
      u32x4 ov; u32x2 ml;
      const int goff = WS_O + ((kvh * 16 + j) * 16 + (lane & 15)) * 64 + G * 16, moff = WS_ST + ((kvh * 2 + (j >> 3)) * 16 + (lane & 15)) * 8;
      __builtin_amdgcn_s_sleep(4);
      for (unsigned spin = 0; spin < SPIN_CAP; ++spin) {
        CBAR();
        ov = ld_sc(rw, goff, 0);
        ml = __builtin_amdgcn_raw_buffer_load_b64(rw, moff, 0, AUX_SC1);
        if (__all(filled(ov) & (max(ml.x, ml.y) != EMPTY))) break;
      }
      const float e = __builtin_amdgcn_exp2f((asf(ml.x) - max8(asf(ml.x))) * 1.4426950408889634f);
      const float f = e * __builtin_amdgcn_rcpf(sum8(e * asf(ml.y)));
      const float o0 = sum8(f * asf(ov.x)), o1 = sum8(f * asf(ov.y)), o2 = sum8(f * asf(ov.z)), o3 = sum8(f * asf(ov.w));
      if ((lane & 7) == 0) *(LAS u32x4*)(L + L_CO + (lane >> 3) * 4) = u32x4{pk(o0, o0), pk(o1, o1), pk(o2, o2), pk(o3, o3)};
      lds_signal(L + F_O);
      TS(7);
    } else if (ww == 1) {
      lds_wait(L + C_INIT, 7);
      if (lane == 0) __builtin_amdgcn_raw_buffer_store_b64(u32x2{(unsigned)tag, (unsigned)(tag >> 32)}, rw, WS_FLAG + c * 8, 0, AUX_SC1);
      lds_wait(L + F_Q, 1);
      for (unsigned spin = 0; spin < SPIN_CAP; ++spin) {
        __builtin_amdgcn_s_sleep(16);
        CBAR();
        const u32x4 f0 = ld_sc(rw, WS_FLAG + lane * 16, 0), f1 = ld_sc(rw, WS_FLAG + lane * 16, 1024);
        const unsigned tl = (unsigned)tag, th = (unsigned)(tag >> 32);
        if (__all((f0.x == tl) & (f0.y == th) & (f0.z == tl) & (f0.w == th) & (f1.x == tl) & (f1.y == th) & (f1.z == tl) & (f1.w == th))) break;
      }
      CBAR();
      lds_signal(L + F_OK);
      TS(4);
      if (c == 0 && lane == 0)
        __builtin_amdgcn_raw_buffer_store_b64(u32x2{(unsigned)tag, (unsigned)(tag >> 32)}, rw, WS_E, 0, AUX_SC1);
      if (!isq) {
        u32x4 p[3];
#pragma unroll
        for (int i = 0; i < 3; ++i) p[i] = ld_sc(rw, WS_GU + gi * 3072 + lane * 16, i * 1024);
#pragma unroll
        for (int i = 0; i < 3; ++i) {
          const f32x4 t = {sum8(asf(p[i].x)), sum8(asf(p[i].y)), sum8(asf(p[i].z)), sum8(asf(p[i].w))};
          if ((lane & 7) == 0) *(LAS f32x4*)(L + L_GU + (i * 8 + (lane >> 3)) * 4) = t;
        }
        __builtin_amdgcn_fence(__ATOMIC_ACQ_REL, "wavefront", "local");
        if (lane < 48) { const float h = rbf(asf(L[L_GU + lane])) * rbf(asf(L[L_GU + 48 + lane])); L[L_CH + lane] = pk(h, h); }
      }
      lds_signal(L + F_H);
      TS(5);
      lds_wait(L + F_V, 1);
#pragma unroll
      for (int i = 0; i < 4; ++i) W3[i] = ld_nt(ro, wo_off, i * (D * 2));
#pragma unroll
      for (int i = 0; i < 6; ++i) W3[4 + i] = ld_nt(rd, wd_off, i * (D * 2));
      __builtin_amdgcn_sched_barrier(0);
    }
  }

  if (w < 4 || w == 5) {
    if (!isq) {
      lds_wait(L + F_H, 1);
      u32x2 ch[3];
#pragma unroll
      for (int i = 0; i < 3; ++i) ch[i] = *(LAS u32x2*)(L + L_CH + sel * 24 + G * 6 + i * 2);
#pragma unroll
      for (int i = 0; i < 6; ++i) { const unsigned t = ch[i >> 1][i & 1]; y = gemv4(t, mask, W3[4 + i], y); }
    }
    TS(6);
    lds_wait(L + F_O, 1);
    const u32x4 co = *(LAS u32x4*)(L + L_CO + (lane >> 3) * 4);
    TS(7);
#pragma unroll
    for (int i = 0; i < 4; ++i) { const unsigned t = co[i]; y = gemv4(t, mask, W3[i], y); }
    st_sc(rw, yoff, 0, payload(y));
    TS(8);
  } else if (c < 72) lds_wait(L + F_O, 1);

  if (c < 72) {
    __builtin_amdgcn_s_sleep(FIN_DELAY);
    u32x4 p0, p1;
    const int off = WS_Y + c * 16384 + w * 2048 + lane * 16;
    for (unsigned spin = 0; spin < SPIN_CAP; ++spin) {
      CBAR();
      p0 = ld_sc(rw, off, 0); p1 = ld_sc(rw, off, 1024);
      if (__all(filled(p0) & filled(p1))) break;
    }
    TS(9);
    f32x4 t = __builtin_bit_cast(f32x4, p0) + __builtin_bit_cast(f32x4, p1);
#pragma unroll
    for (int i = 0; i < 4; ++i) { float u = t[i]; u += dpp<ROR4>(u); u += dpp<ROR8>(u); t[i] = sum_x32(sum_x16(u)); }
    if (lane < 4) *(LAS f32x4*)(L + L_FIN + w * 16 + lane * 4) = t;
    __builtin_amdgcn_fence(__ATOMIC_RELEASE, "workgroup", "local");
    unsigned old = 0;
    if (lane == 0) old = __hip_atomic_fetch_add(L + C_FIN, 1u, __ATOMIC_RELAXED, __HIP_MEMORY_SCOPE_WORKGROUP);
    if (__builtin_amdgcn_readfirstlane(old) == 7) {
      __builtin_amdgcn_fence(__ATOMIC_ACQUIRE, "workgroup", "local");
      if (lane < 4) {
        f32x4 r = *(LAS f32x4*)(L + L_FIN + lane * 4);
#pragma unroll
        for (int i = 1; i < 8; ++i) r += *(LAS f32x4*)(L + L_FIN + i * 16 + lane * 4);
        const u32x2 xr = *(LAS u32x2*)(L + L_XR + lane * 2);
        const int sg = c >> 4, g2 = sg < 4 ? (c >> 2) & 3 : (c >> 1) & 3, mm = sg < 4 ? c & 3 : c & 1;
        *(u32x2*)(a.out + (g2 >> 1) * D + 128 * sg + 8 * (4 * mm + lane) + 4 * (g2 & 1)) =
            u32x2{pk(lo(xr.x) + r.x, hi(xr.x) + r.y), pk(lo(xr.y) + r.z, hi(xr.y) + r.w)};
      }
    }
    TS(10);
  }
  TSFLUSH();
}

}

extern "C" void kernel_launch(void* const* d_in, const int* in_sizes, int n_in, void* d_out, int out_size, void* d_ws, size_t ws_size,
                              hipStream_t stream) {
  Args a;
  a.x = (const u16*)d_in[0]; a.k = (const u16*)d_in[1]; a.v = (const u16*)d_in[2]; a.wq = (const u16*)d_in[3]; a.wo = (const u16*)d_in[4];
  a.wg = (const u16*)d_in[6]; a.wu = (const u16*)d_in[7]; a.wd = (const u16*)d_in[8];
  a.out = (u16*)d_out; a.ws = (unsigned char*)d_ws;
  fused_decode<<<NWG, NTHR, 0, stream>>>(a);
}
```

```cpp
#include <hip/hip_runtime.h>
#include <hip/hip_bf16.h>
#include <stdint.h>

namespace {

typedef unsigned short u16;
typedef unsigned long long u64;
typedef unsigned u32x2 __attribute__((ext_vector_type(2)));
typedef unsigned u32x3 __attribute__((ext_vector_type(3)));
typedef unsigned u32x4 __attribute__((ext_vector_type(4)));
typedef float f32x2 __attribute__((ext_vector_type(2)));
typedef float f32x4 __attribute__((ext_vector_type(4)));
typedef __bf16 bf16x2 __attribute__((ext_vector_type(2)));
typedef __bf16 bf16x8 __attribute__((ext_vector_type(8)));
typedef __amdgpu_buffer_rsrc_t rsrc_t;
#define LAS __attribute__((address_space(3)))

constexpr int D = 576, SK = 4096, DH = 128, F = 4608, NQ = 4096;
constexpr int NWG = 256, NTHR = 512;
constexpr int AUX_NT = 2, AUX_SC1 = 16;
constexpr int OOB = 0x7ffffff0;
constexpr unsigned EMPTY = 0xFFFFFFFFu;
constexpr unsigned SPIN_CAP = 1u << 16;
#define WIN 8
#define FIN_DELAY 4

constexpr int WS_E = 0;
constexpr int WS_FLAG = 4096;
constexpr int WS_ST = 8192;
constexpr int WS_Q = 16384;
constexpr int WS_O = WS_Q + 65536;
constexpr int WS_GU = WS_O + 262144;
constexpr int WS_Y = WS_GU + 192 * 3072;
constexpr int WS_END = WS_Y + 72 * 16384;

constexpr int L_CNT = 0;
enum { C_INIT = 0, C_QP, F_Q, C_ST, C_D, F_OK, F_H, F_O, F_V, C_FIN, N_CNT = 16 };
constexpr int L_Q = 16;
constexpr int L_ST = L_Q + 128;
constexpr int L_CH = L_ST + 16;
constexpr int L_CO = L_CH + 48;
constexpr int L_XR = L_CO + 32;
constexpr int L_GU = L_XR + 8;
constexpr int L_FIN = L_GU + 96;
constexpr int L_D = L_FIN + 128;
constexpr int L_QP = L_D + 1024;
constexpr int L_END = L_QP + 1024;

struct Args { const u16 *x, *k, *v, *wq, *wo, *wg, *wu, *wd; u16* out; unsigned char* ws; };

template <int I> struct IC { static constexpr int value = I; };
template <int N, int I = 0, class Fn> __device__ __forceinline__ void static_for(Fn&& f) {
  if constexpr (I < N) { f(IC<I>{}); static_for<N, I + 1>(f); }
}
__device__ __forceinline__ rsrc_t mk(const void* p, int bytes) { return __builtin_amdgcn_make_buffer_rsrc((void*)p, 0, bytes, 0x00020000); }
__device__ __forceinline__ u32x4 ld_nt(rsrc_t r, int voff, int soff) { return __builtin_amdgcn_raw_buffer_load_b128(r, voff, soff, AUX_NT); }
__device__ __forceinline__ u32x4 ld_sc(rsrc_t r, int voff, int soff) { return __builtin_amdgcn_raw_buffer_load_b128(r, voff, soff, AUX_SC1); }
__device__ __forceinline__ void st_sc(rsrc_t r, int voff, int soff, u32x4 v) { __builtin_amdgcn_raw_buffer_store_b128(v, r, voff, soff, AUX_SC1); }
__device__ __forceinline__ float asf(unsigned u) { return __builtin_bit_cast(float, u); }
__device__ __forceinline__ unsigned asu(float f) { return __builtin_bit_cast(unsigned, f); }
__device__ __forceinline__ float lo(unsigned v) { return asf(v << 16); }
__device__ __forceinline__ float hi(unsigned v) { return asf(v & 0xffff0000u); }
__device__ __forceinline__ unsigned pk(float a, float b) { const f32x2 v = {a, b}; return __builtin_bit_cast(unsigned, __builtin_convertvector(v, bf16x2)); }
__device__ __forceinline__ float rbf(float a) { return lo(pk(a, a)); }
__device__ __forceinline__ float dot2(unsigned a, unsigned b, float c) {
  return __builtin_amdgcn_fdot2_f32_bf16(__builtin_bit_cast(bf16x2, a), __builtin_bit_cast(bf16x2, b), c, false);
}
template <int CTRL> __device__ __forceinline__ float dpp(float x) {
  return asf(__builtin_amdgcn_update_dpp(0, (int)asu(x), CTRL, 0xf, 0xf, true));
}
constexpr int XOR1 = 0xB1, XOR2 = 0x4E, HMIR = 0x141, MIR = 0x140, ROR4 = 0x124, ROR8 = 0x128;
__device__ __forceinline__ float sum8(float a) { a += dpp<XOR1>(a); a += dpp<XOR2>(a); return a + dpp<HMIR>(a); }
__device__ __forceinline__ float max8(float a) { a = fmaxf(a, dpp<XOR1>(a)); a = fmaxf(a, dpp<XOR2>(a)); return fmaxf(a, dpp<HMIR>(a)); }
__device__ __forceinline__ float sum_x16(float a) { auto s = __builtin_amdgcn_permlane16_swap(asu(a), asu(a), false, false); return asf(s[0]) + asf(s[1]); }
__device__ __forceinline__ float sum_x32(float a) { auto s = __builtin_amdgcn_permlane32_swap(asu(a), asu(a), false, false); return asf(s[0]) + asf(s[1]); }
__device__ __forceinline__ float max_x16(float a) { auto s = __builtin_amdgcn_permlane16_swap(asu(a), asu(a), false, false); return fmaxf(asf(s[0]), asf(s[1])); }
__device__ __forceinline__ float max_x32(float a) { auto s = __builtin_amdgcn_permlane32_swap(asu(a), asu(a), false, false); return fmaxf(asf(s[0]), asf(s[1])); }
__device__ __forceinline__ f32x4 gemv4(unsigned coef2, const u32x4& mask, const u32x4& w, f32x4 acc) {
  const u32x4 a = {coef2 & mask.x, coef2 & mask.y, coef2 & mask.z, coef2 & mask.w};
  return __builtin_amdgcn_mfma_f32_16x16x32_bf16(__builtin_bit_cast(bf16x8, a), __builtin_bit_cast(bf16x8, w), acc, 0, 0, 0);
}
__device__ __forceinline__ u32x4 payload(f32x4 v) {
  return u32x4{min(asu(v.x), EMPTY - 1), min(asu(v.y), EMPTY - 1), min(asu(v.z), EMPTY - 1), min(asu(v.w), EMPTY - 1)};
}
__device__ __forceinline__ bool filled(const u32x4& v) { return max(max(v.x, v.y), max(v.z, v.w)) != EMPTY; }
#define CBAR() asm volatile("" ::: "memory")
#define DRAIN_VM() asm volatile("s_waitcnt vmcnt(0)" ::: "memory")
template <int N> __device__ __forceinline__ void throttle() { asm volatile("s_waitcnt vmcnt(%0)" ::"i"(N) : "memory"); }
__device__ __forceinline__ void lds_signal(LAS unsigned* p) {
  __builtin_amdgcn_fence(__ATOMIC_RELEASE, "workgroup", "local");
  if ((threadIdx.x & 63) == 0) __hip_atomic_fetch_add(p, 1u, __ATOMIC_RELAXED, __HIP_MEMORY_SCOPE_WORKGROUP);
  CBAR();
}
__device__ __forceinline__ void lds_wait(LAS unsigned* p, unsigned n) {
  while ((unsigned)__builtin_amdgcn_readfirstlane(__hip_atomic_load(p, __ATOMIC_RELAXED, __HIP_MEMORY_SCOPE_WORKGROUP)) < n) __builtin_amdgcn_s_sleep(1);
  __builtin_amdgcn_fence(__ATOMIC_ACQUIRE, "workgroup", "local");
}
#define TS(i)
#define TSDECL
#define TSFLUSH()

__device__ __forceinline__ void normalize18(const u32x3 (&r)[3], float m, float sd, float rs, unsigned (&o)[18]) {
#pragma unroll
  for (int i = 0; i < 9; ++i) {
    const unsigned t = r[i / 3][i % 3];
    const unsigned d = pk(lo(t) - m, hi(t) - m);
    const float d0 = lo(d), d1 = hi(d);
    float q0 = d0 * rs, q1 = d1 * rs;
    q0 = fmaf(fmaf(-q0, sd, d0), rs, q0);
    q1 = fmaf(fmaf(-q1, sd, d1), rs, q1);
    o[2 * i] = pk(q0, q0);
    o[2 * i + 1] = pk(q1, q1);
  }
}

__global__ void __launch_bounds__(NTHR, 2) fused_decode(Args a) {
  __shared__ __attribute__((aligned(16))) unsigned lds_raw[L_END];
  LAS unsigned* L = (LAS unsigned*)lds_raw;
  TSDECL;
  TS(0);
  const int lane = threadIdx.x & 63, w = __builtin_amdgcn_readfirstlane(threadIdx.x >> 6);
  const int c = blockIdx.x, s = c & 7, pair = c >> 3, bb = pair & 1, kvh = pair >> 1;
  const int sr = (s + pair) & 7;
  const bool isq = (sr & 3) == 0;
  const int qi = pair * 2 + (sr >> 2), gi = pair * 6 + (sr >> 2) * 3 + (sr & 3) - 1;
  const int G = lane >> 4, cc = lane & 15, sel = (lane >> 3) & 1;
  const rsrc_t rw = mk(a.ws, WS_END);
  const u32x4 mask = {(lane & 7) == 0 ? 0xffffu : (lane & 7) == 1 ? 0xffff0000u : 0u, (lane & 7) == 2 ? 0xffffu : (lane & 7) == 3 ? 0xffff0000u : 0u,
                      (lane & 7) == 4 ? 0xffffu : (lane & 7) == 5 ? 0xffff0000u : 0u, (lane & 7) == 6 ? 0xffffu : (lane & 7) == 7 ? 0xffff0000u : 0u};
  const u32x4 empty4 = {EMPTY, EMPTY, EMPTY, EMPTY};
  const int j = bb * 8 + s;
  const int seg = w < 4 ? w : 4;
  const bool segok = seg < 4 || cc < 8;
  const int yoff = segok ? WS_Y + ((seg * 16 + (seg < 4 ? (lane >> 2) : G * 2 + (cc >> 2))) * 256 + c) * 64 + (lane & 3) * 16 : OOB;
  const int wo_off = segok ? (kvh * 256 + (j >> 3) * 128 + 8 * (4 * (j & 3) + G) + 4 * ((j >> 2) & 1)) * (D * 2) + seg * 256 + cc * 16 : OOB;
  const int wd_off = segok && !isq ? (gi * 24 + 6 * G) * (D * 2) + seg * 256 + cc * 16 : OOB;
  const rsrc_t ro = mk(a.wo, NQ * D * 2), rd = mk(a.wd, F * D * 2);
  u32x4 W3[10];
  f32x4 y = {0.f, 0.f, 0.f, 0.f};

  if (w < 4) {
    if (w == 0 && lane < N_CNT) L[L_CNT + lane] = 0u;
    st_sc(rw, yoff, 0, empty4);
    const size_t kvbase = ((size_t)(bb * 16 + kvh) * SK + s * 512) * DH;
    const rsrc_t rk = mk(a.k + kvbase, 512 * DH * 2), rv = mk(a.v + kvbase, 512 * DH * 2);
    const int voff = w * 1024 + lane * 16;
    asm volatile("s_waitcnt lgkmcnt(0)" ::: "memory");
    __builtin_amdgcn_s_barrier();
    CBAR();
    TS(1);
    u32x4 K[32], V[32], qa, qb;
    float sc[32];
    f32x4 acc = {0.f, 0.f, 0.f, 0.f};
    float mx = -INFINITY, lsum = 0.f, mneg = 0.f;
    constexpr int QK0 = 32, PV0 = QK0 + 16;
    static_for<74>([&](auto ic) {
      constexpr int n = ic.value;
      if constexpr (n >= WIN) throttle<WIN - 1>();
      if constexpr (n < 32) K[n] = ld_nt(rk, voff, n * 4096);
      else if constexpr (n < 64) V[n - 32] = ld_nt(rv, voff, (n - 32) * 4096);
      else if constexpr (n < 68) W3[n - 64] = ld_nt(ro, wo_off, (n - 64) * (D * 2));
      else W3[n - 64] = ld_nt(rd, wd_off, (n - 68) * (D * 2));
      if constexpr (n == WIN) lds_signal(L + C_INIT);
      if constexpr (n == 60) { if (w == 0) lds_signal(L + F_V); }
      if constexpr (n == QK0) {
        TS(2);
        lds_wait(L + F_Q, 1);
        qa = *(LAS u32x4*)(L + L_Q + sel * 64 + cc * 4);
        qb = *(LAS u32x4*)(L + L_Q + (sel ^ 1) * 64 + cc * 4);
        TS(3);
      }
      if constexpr (n >= QK0 && n < PV0) {
        constexpr int i = 2 * (n - QK0);
        float t[2];
#pragma unroll
        for (int e = 0; e < 2; ++e) {
          float u = 0.f, v = 0.f;
#pragma unroll
          for (int d = 0; d < 4; ++d) { const unsigned kk = K[i + e][d], q0 = qa[d], q1 = qb[d]; u = dot2(kk, q0, u); v = dot2(kk, q1, v); }
          t[e] = sum8(u + dpp<ROR8>(v));
        }
        const unsigned r1 = pk(t[0], t[1]);
        const unsigned r2 = pk(lo(r1) * 0.08838834764831845f, hi(r1) * 0.08838834764831845f);
        sc[i] = lo(r2); sc[i + 1] = hi(r2);
        mx = fmaxf(mx, fmaxf(sc[i], sc[i + 1]));
      }
      if constexpr (n == PV0 - 1) {
        mx = max_x32(max_x16(mx));
        mneg = -mx * 1.4426950408889634f;
#pragma unroll
        for (int i = 0; i < 32; ++i) { sc[i] = __builtin_amdgcn_exp2f(fmaf(sc[i], 1.4426950408889634f, mneg)); lsum += sc[i]; }
        lsum = sum_x32(sum_x16(lsum));
        if ((lane & 0x37) == 0) *(LAS f32x2*)(L + L_ST + w * 4 + sel * 2) = f32x2{mx, lsum};
        lds_signal(L + C_ST);
        TS(4);
      }
      if constexpr (n >= PV0) {
        constexpr int done = (n - 1 - PV0) * 2 + 2 < n - 1 - 32 - WIN + 1 ? (n - 1 - PV0) * 2 + 2 : n - 1 - 32 - WIN + 1;
        constexpr int upto = (n - PV0) * 2 + 2 < n - 32 - WIN + 1 ? (n - PV0) * 2 + 2 : n - 32 - WIN + 1;
        static_for<32>([&](auto jc) {
          constexpr int i = jc.value;
          if constexpr (i >= (n == PV0 ? 0 : done) && i < upto) acc = gemv4(pk(sc[i], sc[i]), mask, V[i], acc);
          if constexpr (i == 31 && i >= done && i < upto) {
            TS(5);
            *(LAS f32x4*)(L + L_D + w * 256 + lane * 4) = acc;
            lds_signal(L + C_D);
          }
        });
      }
      __builtin_amdgcn_sched_barrier(0);
    });
    static_assert(73 - 32 - WIN + 1 >= 32, "all V loads are consumed inside the step list");
  } else {
    __builtin_amdgcn_s_setprio(2);
    const int ww = w - 4;
    const rsrc_t rx = mk(a.x, 2 * D * 2);
    const u32x2 ev = __builtin_amdgcn_raw_buffer_load_b64(rw, WS_E, 0, AUX_SC1);
    u32x3 xs[3], x0[3], x1[3];
#pragma unroll
    for (int i = 0; i < 3; ++i) xs[i] = __builtin_amdgcn_raw_buffer_load_b96(rx, lane * 36 + i * 12, 0, 0);
    const int sl = isq ? ww : gi * 3 + ww;
    const bool act = isq || ww < 3;
    const int r0 = isq ? 144 * ww + 18 * (2 * G + sel) : 72 * (sl & 7) + 18 * G;
    const int blk = sl >> 3, mat = blk >= 36, cb = blk - 36 * mat;
#pragma unroll
    for (int i = 0; i < 3; ++i) {
      x0[i] = __builtin_amdgcn_raw_buffer_load_b96(rx, (isq ? 0 : sel * (D * 2)) + r0 * 2 + i * 12, 0, 0);
      x1[i] = __builtin_amdgcn_raw_buffer_load_b96(rx, isq ? D * 2 + r0 * 2 + i * 12 : OOB, 0, 0);
    }
    const int rstride = isq ? NQ * 2 : F * 2;
    const rsrc_t r1 = mk(isq ? a.wq : mat ? a.wu : a.wg, D * rstride);
    const int woff = !act ? OOB : isq ? r0 * rstride + qi * 128 + (lane & 7) * 16 : r0 * rstride + cb * 256 + cc * 16;
    u32x4 Wt[18];
#pragma unroll
    for (int i = 0; i < 18; ++i) Wt[i] = ld_nt(r1, woff, i * rstride);
    __builtin_amdgcn_sched_barrier(0);
    __builtin_amdgcn_s_barrier();
    CBAR();
    TS(1);
    const int ooff = WS_O + ((kvh * 16 + (lane >> 2)) * 16 + j) * 64 + (lane & 3) * 16;
    const int stoff = (lane & 31) == 0 ? WS_ST + ((kvh * 2 + (lane >> 5)) * 16 + j) * 8 : OOB;
    if (ww == 0) {
      st_sc(rw, ooff, 0, empty4);
      __builtin_amdgcn_raw_buffer_store_b64(u32x2{EMPTY, EMPTY}, rw, stoff, 0, AUX_SC1);
    } else if (ww == 1) st_sc(rw, yoff, 0, empty4);
    float sm = 0.f;
#pragma unroll
    for (int i = 0; i < 9; ++i) { const unsigned t = xs[i / 3][i % 3]; sm += lo(t) + hi(t); }
    sm = sum8(sm); sm += dpp<MIR>(sm); sm = sum_x16(sm);
    const float mean = sm * (1.f / D);
    float sv = 0.f;
#pragma unroll
    for (int i = 0; i < 9; ++i) { const unsigned t = xs[i / 3][i % 3]; const float d0 = lo(t) - mean, d1 = hi(t) - mean; sv = fmaf(d0, d0, fmaf(d1, d1, sv)); }
    sv = sum8(sv); sv += dpp<MIR>(sv); sv = sum_x16(sv);
    const float mb = rbf(mean), sd = rbf(sqrtf(rbf(rbf(sv * (1.f / D)) + 1e-5f))), rs = __builtin_amdgcn_rcpf(sd);
    const float m0 = asf(__builtin_amdgcn_readlane(asu(mb), 0)), m1 = asf(__builtin_amdgcn_readlane(asu(mb), 32));
    const float s0 = asf(__builtin_amdgcn_readlane(asu(sd), 0)), s1 = asf(__builtin_amdgcn_readlane(asu(sd), 32));
    const float i0 = asf(__builtin_amdgcn_readlane(asu(rs), 0)), i1 = asf(__builtin_amdgcn_readlane(asu(rs), 32));
    const u64 tag = (((u64)(unsigned)__builtin_amdgcn_readfirstlane(ev.y) << 32) | (unsigned)__builtin_amdgcn_readfirstlane(ev.x)) + 1;
    const unsigned tag_lo16 = (unsigned)tag << 16, tag_hi = (unsigned)(tag >> 16);
    if (isq) {
      unsigned c0[18], c1[18];
      normalize18(x0, m0, s0, i0, c0);
      normalize18(x1, m1, s1, i1, c1);
      f32x4 d0 = {0.f, 0.f, 0.f, 0.f}, d1 = d0;
#pragma unroll
      for (int i = 0; i < 18; ++i) { d0 = gemv4(c0[i], mask, Wt[i], d0); d1 = gemv4(c1[i], mask, Wt[i], d1); }
      TS(2);
      if (sel == (lane >> 5)) {
        LAS f32x4* qp = (LAS f32x4*)(L + L_QP) + ww * 64 + sel * 16 + (G & 1) * 8 + (lane & 7);
        qp[0] = d0; qp[32] = d1;
      }
      lds_signal(L + C_QP);
      if (ww == 0) {
        lds_wait(L + C_QP, 4);
        const int rr = lane >> 5, b = G & 1;
        f32x2 t = {0.f, 0.f};
#pragma unroll
        for (int i = 0; i < 8; ++i) t += *(LAS f32x2*)(L + L_QP + ((i >> 1) * 64 + b * 32 + (i & 1) * 16 + cc) * 4 + rr * 2);
        const unsigned qq = pk(t.x, t.y);
        st_sc(rw, WS_Q + (((qi >> 2) * 2 + b) * 256 + (qi & 3) * 64 + (lane & 7) * 8 + sel * 4 + rr * 2) * 8, 0,
              u32x4{(qq & 0xffffu) | tag_lo16, tag_hi, (qq >> 16) | tag_lo16, tag_hi});
      }
    } else if (act) {
      unsigned c0[18];
      normalize18(x0, sel ? m1 : m0, sel ? s1 : s0, sel ? i1 : i0, c0);
      f32x4 d0 = {0.f, 0.f, 0.f, 0.f};
#pragma unroll
      for (int i = 0; i < 18; ++i) d0 = gemv4(c0[i], mask, Wt[i], d0);
      TS(2);
      const int f4 = cb * 32 + cc * 2 + (G & 1), cons = f4 / 6;
      st_sc(rw, WS_GU + cons * 3072 + ((mat * 12 + (G >> 1) * 6 + (f4 - cons * 6)) * 8 + (sl & 7)) * 16, 0, __builtin_bit_cast(u32x4, d0));
    }
    if (ww == 3 && c < 72 && lane < 4) {
      const int sg = c >> 4, g2 = sg < 4 ? (c >> 2) & 3 : (c >> 1) & 3, mm = sg < 4 ? c & 3 : c & 1;
      *(LAS u32x2*)(L + L_XR + lane * 2) = __builtin_amdgcn_raw_buffer_load_b64(rx, ((g2 >> 1) * D + 128 * sg + 8 * (4 * mm + lane) + 4 * (g2 & 1)) * 2, 0, 0);
    }
    DRAIN_VM();
    TS(3);
    if (ww != 1) lds_signal(L + C_INIT);

    if (ww == 0) {
      u32x4 g0, g1;
      const int qoff = WS_Q + (kvh * 2 + bb) * 2048 + lane * 16;
      for (unsigned spin = 0; spin < SPIN_CAP; ++spin) {
        CBAR();
        g0 = ld_sc(rw, qoff, 0); g1 = ld_sc(rw, qoff, 1024);
        const bool ok = ((g0.x & 0xffff0000u) == tag_lo16) & (g0.y == tag_hi) & ((g0.z & 0xffff0000u) == tag_lo16) & (g0.w == tag_hi) &
                        ((g1.x & 0xffff0000u) == tag_lo16) & (g1.y == tag_hi) & ((g1.z & 0xffff0000u) == tag_lo16) & (g1.w == tag_hi);
        if (__all(ok)) break;
        __builtin_amdgcn_s_sleep(8);
      }
      L[L_Q + lane] = (g0.x & 0xffffu) | (g0.z << 16);
      L[L_Q + 64 + lane] = (g1.x & 0xffffu) | (g1.z << 16);
      lds_signal(L + F_Q);
      TS(4);
      lds_wait(L + C_ST, 4);
      f32x2 st[4];
#pragma unroll
      for (int i = 0; i < 4; ++i) st[i] = *(LAS f32x2*)(L + L_ST + i * 4 + (lane >> 5) * 2);
      const float M = fmaxf(fmaxf(st[0].x, st[1].x), fmaxf(st[2].x, st[3].x));
      float fw[4], Ls = 0.f;
#pragma unroll
      for (int i = 0; i < 4; ++i) { fw[i] = __builtin_amdgcn_exp2f((st[i].x - M) * 1.4426950408889634f); Ls = fmaf(fw[i], st[i].y, Ls); }
      __builtin_amdgcn_raw_buffer_store_b64(u32x2{min(asu(M), EMPTY - 1), min(asu(Ls), EMPTY - 1)}, rw, stoff, 0, AUX_SC1);
      lds_wait(L + F_OK, 1);
      TS(5);
      lds_wait(L + C_D, 4);
      f32x4 o = {0.f, 0.f, 0.f, 0.f};
#pragma unroll
      for (int i = 0; i < 4; ++i) o += fw[i] * *(LAS f32x4*)(L + L_D + i * 256 + lane * 4);
      st_sc(rw, ooff, 0, payload(o));
      TS(6);
      u32x4 ov; u32x2 ml;
      const int goff = WS_O + ((kvh * 16 + j) * 16 + (lane & 15)) * 64 + G * 16, moff = WS_ST + ((kvh * 2 + (j >> 3)) * 16 + (lane & 15)) * 8;
      __builtin_amdgcn_s_sleep(4);
      for (unsigned spin = 0; spin < SPIN_CAP; ++spin) {
        CBAR();
        ov = ld_sc(rw, goff, 0);
        ml = __builtin_amdgcn_raw_buffer_load_b64(rw, moff, 0, AUX_SC1);
        if (__all(filled(ov) & (max(ml.x, ml.y) != EMPTY))) break;
      }
      const float e = __builtin_amdgcn_exp2f((asf(ml.x) - max8(asf(ml.x))) * 1.4426950408889634f);
      const float f = e * __builtin_amdgcn_rcpf(sum8(e * asf(ml.y)));
      const float o0 = sum8(f * asf(ov.x)), o1 = sum8(f * asf(ov.y)), o2 = sum8(f * asf(ov.z)), o3 = sum8(f * asf(ov.w));
      if ((lane & 7) == 0) *(LAS u32x4*)(L + L_CO + (lane >> 3) * 4) = u32x4{pk(o0, o0), pk(o1, o1), pk(o2, o2), pk(o3, o3)};
      lds_signal(L + F_O);
      TS(7);
    } else if (ww == 1) {
      lds_wait(L + C_INIT, 7);
      if (lane == 0) __builtin_amdgcn_raw_buffer_store_b64(u32x2{(unsigned)tag, (unsigned)(tag >> 32)}, rw, WS_FLAG + c * 8, 0, AUX_SC1);
      lds_wait(L + F_Q, 1);
      for (unsigned spin = 0; spin < SPIN_CAP; ++spin) {
        __builtin_amdgcn_s_sleep(16);
        CBAR();
        const u32x4 f0 = ld_sc(rw, WS_FLAG + lane * 16, 0), f1 = ld_sc(rw, WS_FLAG + lane * 16, 1024);
        const unsigned tl = (unsigned)tag, th = (unsigned)(tag >> 32);
        if (__all((f0.x == tl) & (f0.y == th) & (f0.z == tl) & (f0.w == th) & (f1.x == tl) & (f1.y == th) & (f1.z == tl) & (f1.w == th))) break;
      }
      CBAR();
      lds_signal(L + F_OK);
      TS(4);
      if (c == 0 && lane == 0)
        __builtin_amdgcn_raw_buffer_store_b64(u32x2{(unsigned)tag, (unsigned)(tag >> 32)}, rw, WS_E, 0, AUX_SC1);
      if (!isq) {
        u32x4 p[3];
#pragma unroll
        for (int i = 0; i < 3; ++i) p[i] = ld_sc(rw, WS_GU + gi * 3072 + lane * 16, i * 1024);
#pragma unroll
        for (int i = 0; i < 3; ++i) {
          const f32x4 t = {sum8(asf(p[i].x)), sum8(asf(p[i].y)), sum8(asf(p[i].z)), sum8(asf(p[i].w))};
          if ((lane & 7) == 0) *(LAS f32x4*)(L + L_GU + (i * 8 + (lane >> 3)) * 4) = t;
        }
        __builtin_amdgcn_fence(__ATOMIC_ACQ_REL, "wavefront", "local");
        if (lane < 48) { const float h = rbf(asf(L[L_GU + lane])) * rbf(asf(L[L_GU + 48 + lane])); L[L_CH + lane] = pk(h, h); }
      }
      lds_signal(L + F_H);
      TS(5);
      lds_wait(L + F_V, 1);
#pragma unroll
      for (int i = 0; i < 4; ++i) W3[i] = ld_nt(ro, wo_off, i * (D * 2));
#pragma unroll
      for (int i = 0; i < 6; ++i) W3[4 + i] = ld_nt(rd, wd_off, i * (D * 2));
      __builtin_amdgcn_sched_barrier(0);
    }
  }

  if (w < 4 || w == 5) {
    if (!isq) {
      lds_wait(L + F_H, 1);
      u32x2 ch[3];
#pragma unroll
      for (int i = 0; i < 3; ++i) ch[i] = *(LAS u32x2*)(L + L_CH + sel * 24 + G * 6 + i * 2);
#pragma unroll
      for (int i = 0; i < 6; ++i) { const unsigned t = ch[i >> 1][i & 1]; y = gemv4(t, mask, W3[4 + i], y); }
    }
    TS(6);
    lds_wait(L + F_O, 1);
    const u32x4 co = *(LAS u32x4*)(L + L_CO + (lane >> 3) * 4);
    TS(7);
#pragma unroll
    for (int i = 0; i < 4; ++i) { const unsigned t = co[i]; y = gemv4(t, mask, W3[i], y); }
    st_sc(rw, yoff, 0, payload(y));
    TS(8);
  } else if (c < 72) lds_wait(L + F_O, 1);

  if (c < 72) {
    __builtin_amdgcn_s_sleep(FIN_DELAY);
    u32x4 p0, p1;
    const int off = WS_Y + c * 16384 + w * 2048 + lane * 16;
    for (unsigned spin = 0; spin < SPIN_CAP; ++spin) {
      CBAR();
      p0 = ld_sc(rw, off, 0); p1 = ld_sc(rw, off, 1024);
      if (__all(filled(p0) & filled(p1))) break;
    }
    TS(9);
    f32x4 t = __builtin_bit_cast(f32x4, p0) + __builtin_bit_cast(f32x4, p1);
#pragma unroll
    for (int i = 0; i < 4; ++i) { float u = t[i]; u += dpp<ROR4>(u); u += dpp<ROR8>(u); t[i] = sum_x32(sum_x16(u)); }
    if (lane < 4) *(LAS f32x4*)(L + L_FIN + w * 16 + lane * 4) = t;
    __builtin_amdgcn_fence(__ATOMIC_RELEASE, "workgroup", "local");
    unsigned old = 0;
    if (lane == 0) old = __hip_atomic_fetch_add(L + C_FIN, 1u, __ATOMIC_RELAXED, __HIP_MEMORY_SCOPE_WORKGROUP);
    if (__builtin_amdgcn_readfirstlane(old) == 7) {
      __builtin_amdgcn_fence(__ATOMIC_ACQUIRE, "workgroup", "local");
      if (lane < 4) {
        f32x4 r = *(LAS f32x4*)(L + L_FIN + lane * 4);
#pragma unroll
        for (int i = 1; i < 8; ++i) r += *(LAS f32x4*)(L + L_FIN + i * 16 + lane * 4);
        const u32x2 xr = *(LAS u32x2*)(L + L_XR + lane * 2);
        const int sg = c >> 4, g2 = sg < 4 ? (c >> 2) & 3 : (c >> 1) & 3, mm = sg < 4 ? c & 3 : c & 1;
        *(u32x2*)(a.out + (g2 >> 1) * D + 128 * sg + 8 * (4 * mm + lane) + 4 * (g2 & 1)) =
            u32x2{pk(lo(xr.x) + r.x, hi(xr.x) + r.y), pk(lo(xr.y) + r.z, hi(xr.y) + r.w)};
      }
    }
    TS(10);
  }
  TSFLUSH();
}

}

namespace probe {
typedef unsigned u32x4 __attribute__((ext_vector_type(4)));
template <int I> struct IC { static constexpr int value = I; };
template <int N, int I = 0, class F> __device__ __forceinline__ void sfor(F&& f) { if constexpr (I < N) { f(IC<I>{}); sfor<N, I + 1>(f); } }
#define RS(p, n) __builtin_amdgcn_make_buffer_rsrc((void*)(p), 0, (n), 0x00020000)
__global__ void k_empty(unsigned* ws) {}
template <int AUX>
__global__ void __launch_bounds__(256) k_stream(const char* k, const char* v, unsigned* ws) {
  const int tid = threadIdx.x, c = blockIdx.x;
  auto rk = RS(k + (size_t)c * 131072, 131072), rv = RS(v + (size_t)c * 131072, 131072);
  u32x4 a[8]; u32x4 acc = {0, 0, 0, 0};
  auto load = [&](auto ic) -> u32x4 { constexpr int i = ic.value;
    if constexpr (i < 32) return __builtin_amdgcn_raw_buffer_load_b128(rk, tid * 16, i * 4096, AUX);
    else return __builtin_amdgcn_raw_buffer_load_b128(rv, tid * 16, (i - 32) * 4096, AUX); };
  sfor<8>([&](auto ic) { a[ic.value] = load(ic); });
  __builtin_amdgcn_sched_barrier(0);
  sfor<64>([&](auto ic) { constexpr int i = ic.value; acc ^= a[i % 8]; asm volatile("" : "+v"(acc));
    if constexpr (i + 8 < 64) a[i % 8] = load(IC<i + 8>{}); __builtin_amdgcn_sched_barrier(0); });
  if ((acc.x ^ acc.y ^ acc.z ^ acc.w) == 0x12345678u) ws[(8 << 20) / 4] = 1;
}
#define R4(x) x x x x
#define R16(x) R4(R4(x))
#define R256(x) R16(R16(x))
__global__ void __launch_bounds__(64) k_code(unsigned* ws) {
  unsigned v = threadIdx.x;
  asm volatile(R16(R256("v_add_u32 %0, 1, %0\n\t")) : "+v"(v));
  if (v == 12345) ws[(8 << 20) / 4] = 2;
}
__global__ void __launch_bounds__(64) k_loop(unsigned* ws) {
  unsigned v = threadIdx.x;
#pragma unroll 1
  for (int i = 0; i < 64; ++i) asm volatile(R4(R16("v_add_u32 %0, 1, %0\n\t")) : "+v"(v));
  if (v == 12345) ws[(8 << 20) / 4] = 2;
}
template <int STRIDE>
__global__ void __launch_bounds__(64) k_chase(const char* k, unsigned* ws) {
  auto rk = RS(k, 33554432);
  unsigned off = blockIdx.x * 128;
#pragma unroll 1
  for (int i = 0; i < 24; ++i) { unsigned t = __builtin_amdgcn_raw_buffer_load_b32(rk, off, 0, 2); off = (off + STRIDE + __builtin_amdgcn_readfirstlane(t & 0 ) ) & 33554431; asm volatile("" : "+v"(off) : "v"(t)); }
  if (off == 12345) ws[(8 << 20) / 4] = 2;
}
}

extern "C" void kernel_launch(void* const* d_in, const int* in_sizes, int n_in, void* d_out, int out_size, void* d_ws, size_t ws_size,
                              hipStream_t stream) {
  Args a;
  a.x = (const u16*)d_in[0]; a.k = (const u16*)d_in[1]; a.v = (const u16*)d_in[2]; a.wq = (const u16*)d_in[3]; a.wo = (const u16*)d_in[4];
  a.wg = (const u16*)d_in[6]; a.wu = (const u16*)d_in[7]; a.wd = (const u16*)d_in[8];
  a.out = (u16*)d_out; a.ws = (unsigned char*)d_ws;
  fused_decode<<<NWG, NTHR, 0, stream>>>(a);
  probe::k_empty<<<1, 64, 0, stream>>>((unsigned*)d_ws);
}
```

```cpp
#include <hip/hip_runtime.h>
#include <hip/hip_bf16.h>
#include <stdint.h>

namespace {

typedef unsigned short u16;
typedef unsigned long long u64;
typedef unsigned u32x2 __attribute__((ext_vector_type(2)));
typedef unsigned u32x3 __attribute__((ext_vector_type(3)));
typedef unsigned u32x4 __attribute__((ext_vector_type(4)));
typedef float f32x2 __attribute__((ext_vector_type(2)));
typedef float f32x4 __attribute__((ext_vector_type(4)));
typedef __bf16 bf16x2 __attribute__((ext_vector_type(2)));
typedef __bf16 bf16x8 __attribute__((ext_vector_type(8)));
typedef __amdgpu_buffer_rsrc_t rsrc_t;
#define LAS __attribute__((address_space(3)))

constexpr int D = 576, SK = 4096, DH = 128, F = 4608, NQ = 4096;
constexpr int NWG = 256, NTHR = 512;
constexpr int AUX_NT = 19, AUX_SC1 = 16;
constexpr int OOB = 0x7ffffff0;
constexpr unsigned EMPTY = 0xFFFFFFFFu;
constexpr unsigned SPIN_CAP = 1u << 16;
#define WIN 8
#define FIN_DELAY 4

constexpr int WS_E = 0;
constexpr int WS_FLAG = 4096;
constexpr int WS_ST = 8192;
constexpr int WS_Q = 16384;
constexpr int WS_O = WS_Q + 65536;
constexpr int WS_GU = WS_O + 262144;
constexpr int WS_Y = WS_GU + 192 * 3072;
constexpr int WS_END = WS_Y + 72 * 16384;

constexpr int L_CNT = 0;
enum { C_INIT = 0, C_QP, F_Q, C_ST, C_D, F_OK, F_H, F_O, F_V, C_FIN, N_CNT = 16 };
constexpr int L_Q = 16;
constexpr int L_ST = L_Q + 128;
constexpr int L_CH = L_ST + 16;
constexpr int L_CO = L_CH + 48;
constexpr int L_XR = L_CO + 32;
constexpr int L_GU = L_XR + 8;
constexpr int L_FIN = L_GU + 96;
constexpr int L_D = L_FIN + 128;
constexpr int L_QP = L_D + 1024;
constexpr int L_END = L_QP + 1024;

struct Args { const u16 *x, *k, *v, *wq, *wo, *wg, *wu, *wd; u16* out; unsigned char* ws; };

template <int I> struct IC { static constexpr int value = I; };
template <int N, int I = 0, class Fn> __device__ __forceinline__ void static_for(Fn&& f) {
  if constexpr (I < N) { f(IC<I>{}); static_for<N, I + 1>(f); }
}
__device__ __forceinline__ rsrc_t mk(const void* p, int bytes) { return __builtin_amdgcn_make_buffer_rsrc((void*)p, 0, bytes, 0x00020000); }
__device__ __forceinline__ u32x4 ld_nt(rsrc_t r, int voff, int soff) { return __builtin_amdgcn_raw_buffer_load_b128(r, voff, soff, AUX_NT); }
__device__ __forceinline__ u32x4 ld_sc(rsrc_t r, int voff, int soff) { return __builtin_amdgcn_raw_buffer_load_b128(r, voff, soff, AUX_SC1); }
__device__ __forceinline__ void st_sc(rsrc_t r, int voff, int soff, u32x4 v) { __builtin_amdgcn_raw_buffer_store_b128(v, r, voff, soff, AUX_SC1); }
__device__ __forceinline__ float asf(unsigned u) { return __builtin_bit_cast(float, u); }
__device__ __forceinline__ unsigned asu(float f) { return __builtin_bit_cast(unsigned, f); }
__device__ __forceinline__ float lo(unsigned v) { return asf(v << 16); }
__device__ __forceinline__ float hi(unsigned v) { return asf(v & 0xffff0000u); }
__device__ __forceinline__ unsigned pk(float a, float b) { const f32x2 v = {a, b}; return __builtin_bit_cast(unsigned, __builtin_convertvector(v, bf16x2)); }
__device__ __forceinline__ float rbf(float a) { return lo(pk(a, a)); }
__device__ __forceinline__ float dot2(unsigned a, unsigned b, float c) {
  return __builtin_amdgcn_fdot2_f32_bf16(__builtin_bit_cast(bf16x2, a), __builtin_bit_cast(bf16x2, b), c, false);
}
template <int CTRL> __device__ __forceinline__ float dpp(float x) {
  return asf(__builtin_amdgcn_update_dpp(0, (int)asu(x), CTRL, 0xf, 0xf, true));
}
constexpr int XOR1 = 0xB1, XOR2 = 0x4E, HMIR = 0x141, MIR = 0x140, ROR4 = 0x124, ROR8 = 0x128;
__device__ __forceinline__ float sum8(float a) { a += dpp<XOR1>(a); a += dpp<XOR2>(a); return a + dpp<HMIR>(a); }
__device__ __forceinline__ float max8(float a) { a = fmaxf(a, dpp<XOR1>(a)); a = fmaxf(a, dpp<XOR2>(a)); return fmaxf(a, dpp<HMIR>(a)); }
__device__ __forceinline__ float sum_x16(float a) { auto s = __builtin_amdgcn_permlane16_swap(asu(a), asu(a), false, false); return asf(s[0]) + asf(s[1]); }
__device__ __forceinline__ float sum_x32(float a) { auto s = __builtin_amdgcn_permlane32_swap(asu(a), asu(a), false, false); return asf(s[0]) + asf(s[1]); }
__device__ __forceinline__ float max_x16(float a) { auto s = __builtin_amdgcn_permlane16_swap(asu(a), asu(a), false, false); return fmaxf(asf(s[0]), asf(s[1])); }
__device__ __forceinline__ float max_x32(float a) { auto s = __builtin_amdgcn_permlane32_swap(asu(a), asu(a), false, false); return fmaxf(asf(s[0]), asf(s[1])); }
__device__ __forceinline__ f32x4 gemv4(unsigned coef2, const u32x4& mask, const u32x4& w, f32x4 acc) {
  const u32x4 a = {coef2 & mask.x, coef2 & mask.y, coef2 & mask.z, coef2 & mask.w};
  return __builtin_amdgcn_mfma_f32_16x16x32_bf16(__builtin_bit_cast(bf16x8, a), __builtin_bit_cast(bf16x8, w), acc, 0, 0, 0);
}
__device__ __forceinline__ u32x4 payload(f32x4 v) {
  return u32x4{min(asu(v.x), EMPTY - 1), min(asu(v.y), EMPTY - 1), min(asu(v.z), EMPTY - 1), min(asu(v.w), EMPTY - 1)};
}
__device__ __forceinline__ bool filled(const u32x4& v) { return max(max(v.x, v.y), max(v.z, v.w)) != EMPTY; }
#define CBAR() asm volatile("" ::: "memory")
#define DRAIN_VM() asm volatile("s_waitcnt vmcnt(0)" ::: "memory")
template <int N> __device__ __forceinline__ void throttle() { asm volatile("s_waitcnt vmcnt(%0)" ::"i"(N) : "memory"); }
__device__ __forceinline__ void lds_signal(LAS unsigned* p) {
  __builtin_amdgcn_fence(__ATOMIC_RELEASE, "workgroup", "local");
  if ((threadIdx.x & 63) == 0) __hip_atomic_fetch_add(p, 1u, __ATOMIC_RELAXED, __HIP_MEMORY_SCOPE_WORKGROUP);
  CBAR();
}
__device__ __forceinline__ void lds_wait(LAS unsigned* p, unsigned n) {
  while ((unsigned)__builtin_amdgcn_readfirstlane(__hip_atomic_load(p, __ATOMIC_RELAXED, __HIP_MEMORY_SCOPE_WORKGROUP)) < n) __builtin_amdgcn_s_sleep(1);
  __builtin_amdgcn_fence(__ATOMIC_ACQUIRE, "workgroup", "local");
}
#define TS(i)
#define TSDECL
#define TSFLUSH()

__device__ __forceinline__ void normalize18(const u32x3 (&r)[3], float m, float sd, float rs, unsigned (&o)[18]) {
#pragma unroll
  for (int i = 0; i < 9; ++i) {
    const unsigned t = r[i / 3][i % 3];
    const unsigned d = pk(lo(t) - m, hi(t) - m);
    const float d0 = lo(d), d1 = hi(d);
    float q0 = d0 * rs, q1 = d1 * rs;
    q0 = fmaf(fmaf(-q0, sd, d0), rs, q0);
    q1 = fmaf(fmaf(-q1, sd, d1), rs, q1);
    o[2 * i] = pk(q0, q0);
    o[2 * i + 1] = pk(q1, q1);
  }
}

__global__ void __launch_bounds__(NTHR, 2) fused_decode(Args a) {
  __shared__ __attribute__((aligned(16))) unsigned lds_raw[L_END];
  LAS unsigned* L = (LAS unsigned*)lds_raw;
  TSDECL;
  TS(0);
  const int lane = threadIdx.x & 63, w = __builtin_amdgcn_readfirstlane(threadIdx.x >> 6);
  const int c = blockIdx.x, s = c & 7, pair = c >> 3, bb = pair & 1, kvh = pair >> 1;
  const int sr = (s + pair) & 7;
  const bool isq = (sr & 3) == 0;
  const int qi = pair * 2 + (sr >> 2), gi = pair * 6 + (sr >> 2) * 3 + (sr & 3) - 1;
  const int G = lane >> 4, cc = lane & 15, sel = (lane >> 3) & 1;
  const rsrc_t rw = mk(a.ws, WS_END);
  const u32x4 mask = {(lane & 7) == 0 ? 0xffffu : (lane & 7) == 1 ? 0xffff0000u : 0u, (lane & 7) == 2 ? 0xffffu : (lane & 7) == 3 ? 0xffff0000u : 0u,
                      (lane & 7) == 4 ? 0xffffu : (lane & 7) == 5 ? 0xffff0000u : 0u, (lane & 7) == 6 ? 0xffffu : (lane & 7) == 7 ? 0xffff0000u : 0u};
  const u32x4 empty4 = {EMPTY, EMPTY, EMPTY, EMPTY};
  const int j = bb * 8 + s;
  const int seg = w < 4 ? w : 4;
  const bool segok = seg < 4 || cc < 8;
  const int yoff = segok ? WS_Y + ((seg * 16 + (seg < 4 ? (lane >> 2) : G * 2 + (cc >> 2))) * 256 + c) * 64 + (lane & 3) * 16 : OOB;
  const int wo_off = segok ? (kvh * 256 + (j >> 3) * 128 + 8 * (4 * (j & 3) + G) + 4 * ((j >> 2) & 1)) * (D * 2) + seg * 256 + cc * 16 : OOB;
  const int wd_off = segok && !isq ? (gi * 24 + 6 * G) * (D * 2) + seg * 256 + cc * 16 : OOB;
  const rsrc_t ro = mk(a.wo, NQ * D * 2), rd = mk(a.wd, F * D * 2);
  u32x4 W3[10];
  f32x4 y = {0.f, 0.f, 0.f, 0.f};

  if (w < 4) {
    if (w == 0 && lane < N_CNT) L[L_CNT + lane] = 0u;
    st_sc(rw, yoff, 0, empty4);
    const size_t kvbase = ((size_t)(bb * 16 + kvh) * SK + s * 512) * DH;
    const rsrc_t rk = mk(a.k + kvbase, 512 * DH * 2), rv = mk(a.v + kvbase, 512 * DH * 2);
    const int voff = w * 1024 + lane * 16;
    asm volatile("s_waitcnt lgkmcnt(0)" ::: "memory");
    __builtin_amdgcn_s_barrier();
    CBAR();
    TS(1);
    u32x4 K[32], V[32], qa, qb;
    float sc[32];
    f32x4 acc = {0.f, 0.f, 0.f, 0.f};
    float mx = -INFINITY, lsum = 0.f, mneg = 0.f;
    constexpr int QK0 = 32, PV0 = QK0 + 16;
    static_for<74>([&](auto ic) {
      constexpr int n = ic.value;
      if constexpr (n >= WIN) throttle<WIN - 1>();
      if constexpr (n < 32) K[n] = ld_nt(rk, voff, n * 4096);
      else if constexpr (n < 64) V[n - 32] = ld_nt(rv, voff, (n - 32) * 4096);
      else if constexpr (n < 68) W3[n - 64] = ld_nt(ro, wo_off, (n - 64) * (D * 2));
      else W3[n - 64] = ld_nt(rd, wd_off, (n - 68) * (D * 2));
      if constexpr (n == WIN) lds_signal(L + C_INIT);
      if constexpr (n == 60) { if (w == 0) lds_signal(L + F_V); }
      if constexpr (n == QK0) {
        TS(2);
        lds_wait(L + F_Q, 1);
        qa = *(LAS u32x4*)(L + L_Q + sel * 64 + cc * 4);
        qb = *(LAS u32x4*)(L + L_Q + (sel ^ 1) * 64 + cc * 4);
        TS(3);
      }
      if constexpr (n >= QK0 && n < PV0) {
        constexpr int i = 2 * (n - QK0);
        float t[2];
#pragma unroll
        for (int e = 0; e < 2; ++e) {
          float u = 0.f, v = 0.f;
#pragma unroll
          for (int d = 0; d < 4; ++d) { const unsigned kk = K[i + e][d], q0 = qa[d], q1 = qb[d]; u = dot2(kk, q0, u); v = dot2(kk, q1, v); }
          t[e] = sum8(u + dpp<ROR8>(v));
        }
        const unsigned r1 = pk(t[0], t[1]);
        const unsigned r2 = pk(lo(r1) * 0.08838834764831845f, hi(r1) * 0.08838834764831845f);
        sc[i] = lo(r2); sc[i + 1] = hi(r2);
        mx = fmaxf(mx, fmaxf(sc[i], sc[i + 1]));
      }
      if constexpr (n == PV0 - 1) {
        mx = max_x32(max_x16(mx));
        mneg = -mx * 1.4426950408889634f;
#pragma unroll
        for (int i = 0; i < 32; ++i) { sc[i] = __builtin_amdgcn_exp2f(fmaf(sc[i], 1.4426950408889634f, mneg)); lsum += sc[i]; }
        lsum = sum_x32(sum_x16(lsum));
        if ((lane & 0x37) == 0) *(LAS f32x2*)(L + L_ST + w * 4 + sel * 2) = f32x2{mx, lsum};
        lds_signal(L + C_ST);
        TS(4);
      }
      if constexpr (n >= PV0) {
        constexpr int done = (n - 1 - PV0) * 2 + 2 < n - 1 - 32 - WIN + 1 ? (n - 1 - PV0) * 2 + 2 : n - 1 - 32 - WIN + 1;
        constexpr int upto = (n - PV0) * 2 + 2 < n - 32 - WIN + 1 ? (n - PV0) * 2 + 2 : n - 32 - WIN + 1;
        static_for<32>([&](auto jc) {
          constexpr int i = jc.value;
          if constexpr (i >= (n == PV0 ? 0 : done) && i < upto) acc = gemv4(pk(sc[i], sc[i]), mask, V[i], acc);
          if constexpr (i == 31 && i >= done && i < upto) {
            TS(5);
            *(LAS f32x4*)(L + L_D + w * 256 + lane * 4) = acc;
            lds_signal(L + C_D);
          }
        });
      }
      __builtin_amdgcn_sched_barrier(0);
    });
    static_assert(73 - 32 - WIN + 1 >= 32, "all V loads are consumed inside the step list");
  } else {
    __builtin_amdgcn_s_setprio(2);
    const int ww = w - 4;
    const rsrc_t rx = mk(a.x, 2 * D * 2);
    const u32x2 ev = __builtin_amdgcn_raw_buffer_load_b64(rw, WS_E, 0, AUX_SC1);
    u32x3 xs[3], x0[3], x1[3];
#pragma unroll
    for (int i = 0; i < 3; ++i) xs[i] = __builtin_amdgcn_raw_buffer_load_b96(rx, lane * 36 + i * 12, 0, 0);
    const int sl = isq ? ww : gi * 3 + ww;
    const bool act = isq || ww < 3;
    const int r0 = isq ? 144 * ww + 18 * (2 * G + sel) : 72 * (sl & 7) + 18 * G;
    const int blk = sl >> 3, mat = blk >= 36, cb = blk - 36 * mat;
#pragma unroll
    for (int i = 0; i < 3; ++i) {
      x0[i] = __builtin_amdgcn_raw_buffer_load_b96(rx, (isq ? 0 : sel * (D * 2)) + r0 * 2 + i * 12, 0, 0);
      x1[i] = __builtin_amdgcn_raw_buffer_load_b96(rx, isq ? D * 2 + r0 * 2 + i * 12 : OOB, 0, 0);
    }
    const int rstride = isq ? NQ * 2 : F * 2;
    const rsrc_t r1 = mk(isq ? a.wq : mat ? a.wu : a.wg, D * rstride);
    const int woff = !act ? OOB : isq ? r0 * rstride + qi * 128 + (lane & 7) * 16 : r0 * rstride + cb * 256 + cc * 16;
    u32x4 Wt[18];
#pragma unroll
    for (int i = 0; i < 18; ++i) Wt[i] = ld_nt(r1, woff, i * rstride);
    __builtin_amdgcn_sched_barrier(0);
    __builtin_amdgcn_s_barrier();
    CBAR();
    TS(1);
    const int ooff = WS_O + ((kvh * 16 + (lane >> 2)) * 16 + j) * 64 + (lane & 3) * 16;
    const int stoff = (lane & 31) == 0 ? WS_ST + ((kvh * 2 + (lane >> 5)) * 16 + j) * 8 : OOB;
    if (ww == 0) {
      st_sc(rw, ooff, 0, empty4);
      __builtin_amdgcn_raw_buffer_store_b64(u32x2{EMPTY, EMPTY}, rw, stoff, 0, AUX_SC1);
    } else if (ww == 1) st_sc(rw, yoff, 0, empty4);
    float sm = 0.f;
#pragma unroll
    for (int i = 0; i < 9; ++i) { const unsigned t = xs[i / 3][i % 3]; sm += lo(t) + hi(t); }
    sm = sum8(sm); sm += dpp<MIR>(sm); sm = sum_x16(sm);
    const float mean = sm * (1.f / D);
    float sv = 0.f;
#pragma unroll
    for (int i = 0; i < 9; ++i) { const unsigned t = xs[i / 3][i % 3]; const float d0 = lo(t) - mean, d1 = hi(t) - mean; sv = fmaf(d0, d0, fmaf(d1, d1, sv)); }
    sv = sum8(sv); sv += dpp<MIR>(sv); sv = sum_x16(sv);
    const float mb = rbf(mean), sd = rbf(sqrtf(rbf(rbf(sv * (1.f / D)) + 1e-5f))), rs = __builtin_amdgcn_rcpf(sd);
    const float m0 = asf(__builtin_amdgcn_readlane(asu(mb), 0)), m1 = asf(__builtin_amdgcn_readlane(asu(mb), 32));
    const float s0 = asf(__builtin_amdgcn_readlane(asu(sd), 0)), s1 = asf(__builtin_amdgcn_readlane(asu(sd), 32));
    const float i0 = asf(__builtin_amdgcn_readlane(asu(rs), 0)), i1 = asf(__builtin_amdgcn_readlane(asu(rs), 32));
    const u64 tag = (((u64)(unsigned)__builtin_amdgcn_readfirstlane(ev.y) << 32) | (unsigned)__builtin_amdgcn_readfirstlane(ev.x)) + 1;
    const unsigned tag_lo16 = (unsigned)tag << 16, tag_hi = (unsigned)(tag >> 16);
    if (isq) {
      unsigned c0[18], c1[18];
      normalize18(x0, m0, s0, i0, c0);
      normalize18(x1, m1, s1, i1, c1);
      f32x4 d0 = {0.f, 0.f, 0.f, 0.f}, d1 = d0;
#pragma unroll
      for (int i = 0; i < 18; ++i) { d0 = gemv4(c0[i], mask, Wt[i], d0); d1 = gemv4(c1[i], mask, Wt[i], d1); }
      TS(2);
      if (sel == (lane >> 5)) {
        LAS f32x4* qp = (LAS f32x4*)(L + L_QP) + ww * 64 + sel * 16 + (G & 1) * 8 + (lane & 7);
        qp[0] = d0; qp[32] = d1;
      }
      lds_signal(L + C_QP);
      if (ww == 0) {
        lds_wait(L + C_QP, 4);
        const int rr = lane >> 5, b = G & 1;
        f32x2 t = {0.f, 0.f};
#pragma unroll
        for (int i = 0; i < 8; ++i) t += *(LAS f32x2*)(L + L_QP + ((i >> 1) * 64 + b * 32 + (i & 1) * 16 + cc) * 4 + rr * 2);
        const unsigned qq = pk(t.x, t.y);
        st_sc(rw, WS_Q + (((qi >> 2) * 2 + b) * 256 + (qi & 3) * 64 + (lane & 7) * 8 + sel * 4 + rr * 2) * 8, 0,
              u32x4{(qq & 0xffffu) | tag_lo16, tag_hi, (qq >> 16) | tag_lo16, tag_hi});
      }
    } else if (act) {
      unsigned c0[18];
      normalize18(x0, sel ? m1 : m0, sel ? s1 : s0, sel ? i1 : i0, c0);
      f32x4 d0 = {0.f, 0.f, 0.f, 0.f};
#pragma unroll
      for (int i = 0; i < 18; ++i) d0 = gemv4(c0[i], mask, Wt[i], d0);
      TS(2);
      const int f4 = cb * 32 + cc * 2 + (G & 1), cons = f4 / 6;
      st_sc(rw, WS_GU + cons * 3072 + ((mat * 12 + (G >> 1) * 6 + (f4 - cons * 6)) * 8 + (sl & 7)) * 16, 0, __builtin_bit_cast(u32x4, d0));
    }
    if (ww == 3 && c < 72 && lane < 4) {
      const int sg = c >> 4, g2 = sg < 4 ? (c >> 2) & 3 : (c >> 1) & 3, mm = sg < 4 ? c & 3 : c & 1;
      *(LAS u32x2*)(L + L_XR + lane * 2) = __builtin_amdgcn_raw_buffer_load_b64(rx, ((g2 >> 1) * D + 128 * sg + 8 * (4 * mm + lane) + 4 * (g2 & 1)) * 2, 0, 0);
    }
    DRAIN_VM();
    TS(3);
    if (ww != 1) lds_signal(L + C_INIT);

    if (ww == 0) {
      u32x4 g0, g1;
      const int qoff = WS_Q + (kvh * 2 + bb) * 2048 + lane * 16;
      for (unsigned spin = 0; spin < SPIN_CAP; ++spin) {
        CBAR();
        g0 = ld_sc(rw, qoff, 0); g1 = ld_sc(rw, qoff, 1024);
        const bool ok = ((g0.x & 0xffff0000u) == tag_lo16) & (g0.y == tag_hi) & ((g0.z & 0xffff0000u) == tag_lo16) & (g0.w == tag_hi) &
                        ((g1.x & 0xffff0000u) == tag_lo16) & (g1.y == tag_hi) & ((g1.z & 0xffff0000u) == tag_lo16) & (g1.w == tag_hi);
        if (__all(ok)) break;
        __builtin_amdgcn_s_sleep(8);
      }
      L[L_Q + lane] = (g0.x & 0xffffu) | (g0.z << 16);
      L[L_Q + 64 + lane] = (g1.x & 0xffffu) | (g1.z << 16);
      lds_signal(L + F_Q);
      TS(4);
      lds_wait(L + C_ST, 4);
      f32x2 st[4];
#pragma unroll
      for (int i = 0; i < 4; ++i) st[i] = *(LAS f32x2*)(L + L_ST + i * 4 + (lane >> 5) * 2);
      const float M = fmaxf(fmaxf(st[0].x, st[1].x), fmaxf(st[2].x, st[3].x));
      float fw[4], Ls = 0.f;
#pragma unroll
      for (int i = 0; i < 4; ++i) { fw[i] = __builtin_amdgcn_exp2f((st[i].x - M) * 1.4426950408889634f); Ls = fmaf(fw[i], st[i].y, Ls); }
      __builtin_amdgcn_raw_buffer_store_b64(u32x2{min(asu(M), EMPTY - 1), min(asu(Ls), EMPTY - 1)}, rw, stoff, 0, AUX_SC1);
      lds_wait(L + F_OK, 1);
      TS(5);
      lds_wait(L + C_D, 4);
      f32x4 o = {0.f, 0.f, 0.f, 0.f};
#pragma unroll
      for (int i = 0; i < 4; ++i) o += fw[i] * *(LAS f32x4*)(L + L_D + i * 256 + lane * 4);
      st_sc(rw, ooff, 0, payload(o));
      TS(6);
      u32x4 ov; u32x2 ml;
      const int goff = WS_O + ((kvh * 16 + j) * 16 + (lane & 15)) * 64 + G * 16, moff = WS_ST + ((kvh * 2 + (j >> 3)) * 16 + (lane & 15)) * 8;
      __builtin_amdgcn_s_sleep(4);
      for (unsigned spin = 0; spin < SPIN_CAP; ++spin) {
        CBAR();
        ov = ld_sc(rw, goff, 0);
        ml = __builtin_amdgcn_raw_buffer_load_b64(rw, moff, 0, AUX_SC1);
        if (__all(filled(ov) & (max(ml.x, ml.y) != EMPTY))) break;
      }
      const float e = __builtin_amdgcn_exp2f((asf(ml.x) - max8(asf(ml.x))) * 1.4426950408889634f);
      const float f = e * __builtin_amdgcn_rcpf(sum8(e * asf(ml.y)));
      const float o0 = sum8(f * asf(ov.x)), o1 = sum8(f * asf(ov.y)), o2 = sum8(f * asf(ov.z)), o3 = sum8(f * asf(ov.w));
      if ((lane & 7) == 0) *(LAS u32x4*)(L + L_CO + (lane >> 3) * 4) = u32x4{pk(o0, o0), pk(o1, o1), pk(o2, o2), pk(o3, o3)};
      lds_signal(L + F_O);
      TS(7);
    } else if (ww == 1) {
      lds_wait(L + C_INIT, 7);
      if (lane == 0) __builtin_amdgcn_raw_buffer_store_b64(u32x2{(unsigned)tag, (unsigned)(tag >> 32)}, rw, WS_FLAG + c * 8, 0, AUX_SC1);
      lds_wait(L + F_Q, 1);
      for (unsigned spin = 0; spin < SPIN_CAP; ++spin) {
        __builtin_amdgcn_s_sleep(16);
        CBAR();
        const u32x4 f0 = ld_sc(rw, WS_FLAG + lane * 16, 0), f1 = ld_sc(rw, WS_FLAG + lane * 16, 1024);
        const unsigned tl = (unsigned)tag, th = (unsigned)(tag >> 32);
        if (__all((f0.x == tl) & (f0.y == th) & (f0.z == tl) & (f0.w == th) & (f1.x == tl) & (f1.y == th) & (f1.z == tl) & (f1.w == th))) break;
      }
      CBAR();
      lds_signal(L + F_OK);
      TS(4);
      if (c == 0 && lane == 0)
        __builtin_amdgcn_raw_buffer_store_b64(u32x2{(unsigned)tag, (unsigned)(tag >> 32)}, rw, WS_E, 0, AUX_SC1);
      if (!isq) {
        u32x4 p[3];
#pragma unroll
        for (int i = 0; i < 3; ++i) p[i] = ld_sc(rw, WS_GU + gi * 3072 + lane * 16, i * 1024);
#pragma unroll
        for (int i = 0; i < 3; ++i) {
          const f32x4 t = {sum8(asf(p[i].x)), sum8(asf(p[i].y)), sum8(asf(p[i].z)), sum8(asf(p[i].w))};
          if ((lane & 7) == 0) *(LAS f32x4*)(L + L_GU + (i * 8 + (lane >> 3)) * 4) = t;
        }
        __builtin_amdgcn_fence(__ATOMIC_ACQ_REL, "wavefront", "local");
        if (lane < 48) { const float h = rbf(asf(L[L_GU + lane])) * rbf(asf(L[L_GU + 48 + lane])); L[L_CH + lane] = pk(h, h); }
      }
      lds_signal(L + F_H);
      TS(5);
      lds_wait(L + F_V, 1);
#pragma unroll
      for (int i = 0; i < 4; ++i) W3[i] = ld_nt(ro, wo_off, i * (D * 2));
#pragma unroll
      for (int i = 0; i < 6; ++i) W3[4 + i] = ld_nt(rd, wd_off, i * (D * 2));
      __builtin_amdgcn_sched_barrier(0);
    }
  }

  if (w < 4 || w == 5) {
    if (!isq) {
      lds_wait(L + F_H, 1);
      u32x2 ch[3];
#pragma unroll
      for (int i = 0; i < 3; ++i) ch[i] = *(LAS u32x2*)(L + L_CH + sel * 24 + G * 6 + i * 2);
#pragma unroll
      for (int i = 0; i < 6; ++i) { const unsigned t = ch[i >> 1][i & 1]; y = gemv4(t, mask, W3[4 + i], y); }
    }
    TS(6);
    lds_wait(L + F_O, 1);
    const u32x4 co = *(LAS u32x4*)(L + L_CO + (lane >> 3) * 4);
    TS(7);
#pragma unroll
    for (int i = 0; i < 4; ++i) { const unsigned t = co[i]; y = gemv4(t, mask, W3[i], y); }
    st_sc(rw, yoff, 0, payload(y));
    TS(8);
  } else if (c < 72) lds_wait(L + F_O, 1);

  if (c < 72) {
    __builtin_amdgcn_s_sleep(FIN_DELAY);
    u32x4 p0, p1;
    const int off = WS_Y + c * 16384 + w * 2048 + lane * 16;
    for (unsigned spin = 0; spin < SPIN_CAP; ++spin) {
      CBAR();
      p0 = ld_sc(rw, off, 0); p1 = ld_sc(rw, off, 1024);
      if (__all(filled(p0) & filled(p1))) break;
    }
    TS(9);
    f32x4 t = __builtin_bit_cast(f32x4, p0) + __builtin_bit_cast(f32x4, p1);
#pragma unroll
    for (int i = 0; i < 4; ++i) { float u = t[i]; u += dpp<ROR4>(u); u += dpp<ROR8>(u); t[i] = sum_x32(sum_x16(u)); }
    if (lane < 4) *(LAS f32x4*)(L + L_FIN + w * 16 + lane * 4) = t;
    __builtin_amdgcn_fence(__ATOMIC_RELEASE, "workgroup", "local");
    unsigned old = 0;
    if (lane == 0) old = __hip_atomic_fetch_add(L + C_FIN, 1u, __ATOMIC_RELAXED, __HIP_MEMORY_SCOPE_WORKGROUP);
    if (__builtin_amdgcn_readfirstlane(old) == 7) {
      __builtin_amdgcn_fence(__ATOMIC_ACQUIRE, "workgroup", "local");
      if (lane < 4) {
        f32x4 r = *(LAS f32x4*)(L + L_FIN + lane * 4);
#pragma unroll
        for (int i = 1; i < 8; ++i) r += *(LAS f32x4*)(L + L_FIN + i * 16 + lane * 4);
        const u32x2 xr = *(LAS u32x2*)(L + L_XR + lane * 2);
        const int sg = c >> 4, g2 = sg < 4 ? (c >> 2) & 3 : (c >> 1) & 3, mm = sg < 4 ? c & 3 : c & 1;
        *(u32x2*)(a.out + (g2 >> 1) * D + 128 * sg + 8 * (4 * mm + lane) + 4 * (g2 & 1)) =
            u32x2{pk(lo(xr.x) + r.x, hi(xr.x) + r.y), pk(lo(xr.y) + r.z, hi(xr.y) + r.w)};
      }
    }
    TS(10);
  }
  TSFLUSH();
}

}

extern "C" void kernel_launch(void* const* d_in, const int* in_sizes, int n_in, void* d_out, int out_size, void* d_ws, size_t ws_size,
                              hipStream_t stream) {
  Args a;
  a.x = (const u16*)d_in[0]; a.k = (const u16*)d_in[1]; a.v = (const u16*)d_in[2]; a.wq = (const u16*)d_in[3]; a.wo = (const u16*)d_in[4];
  a.wg = (const u16*)d_in[6]; a.wu = (const u16*)d_in[7]; a.wd = (const u16*)d_in[8];
  a.out = (u16*)d_out; a.ws = (unsigned char*)d_ws;
  fused_decode<<<NWG, NTHR, 0, stream>>>(a);
}
```

```cpp
#include <hip/hip_runtime.h>
#include <hip/hip_bf16.h>
#include <stdint.h>

namespace {

typedef unsigned short u16;
typedef unsigned long long u64;
typedef unsigned u32x2 __attribute__((ext_vector_type(2)));
typedef unsigned u32x3 __attribute__((ext_vector_type(3)));
typedef unsigned u32x4 __attribute__((ext_vector_type(4)));
typedef float f32x2 __attribute__((ext_vector_type(2)));
typedef float f32x4 __attribute__((ext_vector_type(4)));
typedef __bf16 bf16x2 __attribute__((ext_vector_type(2)));
typedef __bf16 bf16x8 __attribute__((ext_vector_type(8)));
typedef __amdgpu_buffer_rsrc_t rsrc_t;
#define LAS __attribute__((address_space(3)))

constexpr int D = 576, SK = 4096, DH = 128, F = 4608, NQ = 4096;
constexpr int NWG = 256, NTHR = 512;
constexpr int AUX_NT = 2, AUX_SC1 = 16;
constexpr int OOB = 0x7ffffff0;
constexpr unsigned EMPTY = 0xFFFFFFFFu;
constexpr unsigned SPIN_CAP = 1u << 16;
#define WIN 8
#define FIN_DELAY 4

constexpr int WS_E = 0;
constexpr int WS_FLAG = 4096;
constexpr int WS_ST = 8192;
constexpr int WS_Q = 16384;
constexpr int WS_O = WS_Q + 65536;
constexpr int WS_GU = WS_O + 262144;
constexpr int WS_Y = WS_GU + 192 * 3072;
constexpr int WS_END = WS_Y + 72 * 16384;

constexpr int L_CNT = 0;
enum { C_INIT = 0, C_QP, F_Q, C_ST, C_D, F_OK, F_H, F_O, F_V, C_FIN, N_CNT = 16 };
constexpr int L_Q = 16;
constexpr int L_ST = L_Q + 128;
constexpr int L_CH = L_ST + 16;
constexpr int L_CO = L_CH + 48;
constexpr int L_XR = L_CO + 32;
constexpr int L_GU = L_XR + 8;
constexpr int L_FIN = L_GU + 96;
constexpr int L_D = L_FIN + 128;
constexpr int L_QP = L_D + 1024;
constexpr int L_END = L_QP + 1024;

struct Args { const u16 *x, *k, *v, *wq, *wo, *wg, *wu, *wd; u16* out; unsigned char* ws; };

template <int I> struct IC { static constexpr int value = I; };
template <int N, int I = 0, class Fn> __device__ __forceinline__ void static_for(Fn&& f) {
  if constexpr (I < N) { f(IC<I>{}); static_for<N, I + 1>(f); }
}
__device__ __forceinline__ rsrc_t mk(const void* p, int bytes) { return __builtin_amdgcn_make_buffer_rsrc((void*)p, 0, bytes, 0x00020000); }
__device__ __forceinline__ u32x4 ld_nt(rsrc_t r, int voff, int soff) { return __builtin_amdgcn_raw_buffer_load_b128(r, voff, soff, AUX_NT); }
__device__ __forceinline__ u32x4 ld_sc(rsrc_t r, int voff, int soff) { return __builtin_amdgcn_raw_buffer_load_b128(r, voff, soff, AUX_SC1); }
__device__ __forceinline__ void st_sc(rsrc_t r, int voff, int soff, u32x4 v) { __builtin_amdgcn_raw_buffer_store_b128(v, r, voff, soff, AUX_SC1); }
__device__ __forceinline__ float asf(unsigned u) { return __builtin_bit_cast(float, u); }
__device__ __forceinline__ unsigned asu(float f) { return __builtin_bit_cast(unsigned, f); }
__device__ __forceinline__ float lo(unsigned v) { return asf(v << 16); }
__device__ __forceinline__ float hi(unsigned v) { return asf(v & 0xffff0000u); }
__device__ __forceinline__ unsigned pk(float a, float b) { const f32x2 v = {a, b}; return __builtin_bit_cast(unsigned, __builtin_convertvector(v, bf16x2)); }
__device__ __forceinline__ float rbf(float a) { return lo(pk(a, a)); }
__device__ __forceinline__ float dot2(unsigned a, unsigned b, float c) {
  return __builtin_amdgcn_fdot2_f32_bf16(__builtin_bit_cast(bf16x2, a), __builtin_bit_cast(bf16x2, b), c, false);
}
template <int CTRL> __device__ __forceinline__ float dpp(float x) {
  return asf(__builtin_amdgcn_update_dpp(0, (int)asu(x), CTRL, 0xf, 0xf, true));
}
constexpr int XOR1 = 0xB1, XOR2 = 0x4E, HMIR = 0x141, MIR = 0x140, ROR4 = 0x124, ROR8 = 0x128;
__device__ __forceinline__ float sum8(float a) { a += dpp<XOR1>(a); a += dpp<XOR2>(a); return a + dpp<HMIR>(a); }
__device__ __forceinline__ float max8(float a) { a = fmaxf(a, dpp<XOR1>(a)); a = fmaxf(a, dpp<XOR2>(a)); return fmaxf(a, dpp<HMIR>(a)); }
__device__ __forceinline__ float sum_x16(float a) { auto s = __builtin_amdgcn_permlane16_swap(asu(a), asu(a), false, false); return asf(s[0]) + asf(s[1]); }
__device__ __forceinline__ float sum_x32(float a) { auto s = __builtin_amdgcn_permlane32_swap(asu(a), asu(a), false, false); return asf(s[0]) + asf(s[1]); }
__device__ __forceinline__ float max_x16(float a) { auto s = __builtin_amdgcn_permlane16_swap(asu(a), asu(a), false, false); return fmaxf(asf(s[0]), asf(s[1])); }
__device__ __forceinline__ float max_x32(float a) { auto s = __builtin_amdgcn_permlane32_swap(asu(a), asu(a), false, false); return fmaxf(asf(s[0]), asf(s[1])); }
__device__ __forceinline__ f32x4 gemv4(unsigned coef2, const u32x4& mask, const u32x4& w, f32x4 acc) {
  const u32x4 a = {coef2 & mask.x, coef2 & mask.y, coef2 & mask.z, coef2 & mask.w};
  return __builtin_amdgcn_mfma_f32_16x16x32_bf16(__builtin_bit_cast(bf16x8, a), __builtin_bit_cast(bf16x8, w), acc, 0, 0, 0);
}
__device__ __forceinline__ u32x4 payload(f32x4 v) {
  return u32x4{min(asu(v.x), EMPTY - 1), min(asu(v.y), EMPTY - 1), min(asu(v.z), EMPTY - 1), min(asu(v.w), EMPTY - 1)};
}
__device__ __forceinline__ bool filled(const u32x4& v) { return max(max(v.x, v.y), max(v.z, v.w)) != EMPTY; }
#define CBAR() asm volatile("" ::: "memory")
#define DRAIN_VM() asm volatile("s_waitcnt vmcnt(0)" ::: "memory")
template <int N> __device__ __forceinline__ void throttle() { asm volatile("s_waitcnt vmcnt(%0)" ::"i"(N) : "memory"); }
__device__ __forceinline__ void lds_signal(LAS unsigned* p) {
  __builtin_amdgcn_fence(__ATOMIC_RELEASE, "workgroup", "local");
  if ((threadIdx.x & 63) == 0) __hip_atomic_fetch_add(p, 1u, __ATOMIC_RELAXED, __HIP_MEMORY_SCOPE_WORKGROUP);
  CBAR();
}
__device__ __forceinline__ void lds_wait(LAS unsigned* p, unsigned n) {
  while ((unsigned)__builtin_amdgcn_readfirstlane(__hip_atomic_load(p, __ATOMIC_RELAXED, __HIP_MEMORY_SCOPE_WORKGROUP)) < n) __builtin_amdgcn_s_sleep(1);
  __builtin_amdgcn_fence(__ATOMIC_ACQUIRE, "workgroup", "local");
}
#define TS(i)
#define TSDECL
#define TSFLUSH()

__device__ __forceinline__ void normalize18(const u32x3 (&r)[3], float m, float sd, float rs, unsigned (&o)[18]) {
#pragma unroll
  for (int i = 0; i < 9; ++i) {
    const unsigned t = r[i / 3][i % 3];
    const unsigned d = pk(lo(t) - m, hi(t) - m);
    const float d0 = lo(d), d1 = hi(d);
    float q0 = d0 * rs, q1 = d1 * rs;
    q0 = fmaf(fmaf(-q0, sd, d0), rs, q0);
    q1 = fmaf(fmaf(-q1, sd, d1), rs, q1);
    o[2 * i] = pk(q0, q0);
    o[2 * i + 1] = pk(q1, q1);
  }
}

__global__ void __launch_bounds__(NTHR, 2) fused_decode(Args a) {
  __shared__ __attribute__((aligned(16))) unsigned lds_raw[L_END];
  LAS unsigned* L = (LAS unsigned*)lds_raw;
  TSDECL;
  TS(0);
  const int lane = threadIdx.x & 63, w = __builtin_amdgcn_readfirstlane(threadIdx.x >> 6);
  const int c = blockIdx.x, s = c & 7, pair = c >> 3, bb = pair & 1, kvh = pair >> 1;
  const int sr = (s + pair) & 7;
  const bool isq = (sr & 3) == 0;
  const int qi = pair * 2 + (sr >> 2), gi = pair * 6 + (sr >> 2) * 3 + (sr & 3) - 1;
  const int G = lane >> 4, cc = lane & 15, sel = (lane >> 3) & 1;
  const rsrc_t rw = mk(a.ws, WS_END);
  const u32x4 mask = {(lane & 7) == 0 ? 0xffffu : (lane & 7) == 1 ? 0xffff0000u : 0u, (lane & 7) == 2 ? 0xffffu : (lane & 7) == 3 ? 0xffff0000u : 0u,
                      (lane & 7) == 4 ? 0xffffu : (lane & 7) == 5 ? 0xffff0000u : 0u, (lane & 7) == 6 ? 0xffffu : (lane & 7) == 7 ? 0xffff0000u : 0u};
  const u32x4 empty4 = {EMPTY, EMPTY, EMPTY, EMPTY};
  const int j = bb * 8 + s;
  const int seg = w < 4 ? w : 4;
  const bool segok = seg < 4 || cc < 8;
  const int yoff = segok ? WS_Y + ((seg * 16 + (seg < 4 ? (lane >> 2) : G * 2 + (cc >> 2))) * 256 + c) * 64 + (lane & 3) * 16 : OOB;
  const int wo_off = segok ? (kvh * 256 + (j >> 3) * 128 + 8 * (4 * (j & 3) + G) + 4 * ((j >> 2) & 1)) * (D * 2) + seg * 256 + cc * 16 : OOB;
  const int wd_off = segok && !isq ? (gi * 24 + 6 * G) * (D * 2) + seg * 256 + cc * 16 : OOB;
  const rsrc_t ro = mk(a.wo, NQ * D * 2), rd = mk(a.wd, F * D * 2);
  u32x4 W3[10];
  f32x4 y = {0.f, 0.f, 0.f, 0.f};

  if (w < 4) {
    if (w == 0 && lane < N_CNT) L[L_CNT + lane] = 0u;
    st_sc(rw, yoff, 0, empty4);
    const size_t kvbase = ((size_t)(bb * 16 + kvh) * SK + s * 512) * DH;
    const rsrc_t rk = mk(a.k + kvbase, 512 * DH * 2), rv = mk(a.v + kvbase, 512 * DH * 2);
    const int voff = w * 1024 + lane * 16;
    asm volatile("s_waitcnt lgkmcnt(0)" ::: "memory");
    __builtin_amdgcn_s_barrier();
    CBAR();
    TS(1);
    u32x4 K[32], V[32], qa, qb;
    float sc[32];
    f32x4 acc = {0.f, 0.f, 0.f, 0.f};
    float mx = -INFINITY, lsum = 0.f, mneg = 0.f;
    constexpr int QK0 = 32, PV0 = QK0 + 16;
    static_for<74>([&](auto ic) {
      constexpr int n = ic.value;
      if constexpr (n >= WIN) throttle<WIN - 1>();
      if constexpr (n < 32) K[n] = ld_nt(rk, voff, n * 4096);
      else if constexpr (n < 64) V[n - 32] = ld_nt(rv, voff, (n - 32) * 4096);
      else if constexpr (n < 68) W3[n - 64] = ld_nt(ro, wo_off, (n - 64) * (D * 2));
      else W3[n - 64] = ld_nt(rd, wd_off, (n - 68) * (D * 2));
      if constexpr (n == WIN) lds_signal(L + C_INIT);
      if constexpr (n == 60) { if (w == 0) lds_signal(L + F_V); }
      if constexpr (n == QK0) {
        TS(2);
        lds_wait(L + F_Q, 1);
        qa = *(LAS u32x4*)(L + L_Q + sel * 64 + cc * 4);
        qb = *(LAS u32x4*)(L + L_Q + (sel ^ 1) * 64 + cc * 4);
        TS(3);
      }
      if constexpr (n >= QK0 && n < PV0) {
        constexpr int i = 2 * (n - QK0);
        float t[2];
#pragma unroll
        for (int e = 0; e < 2; ++e) {
          float u = 0.f, v = 0.f;
#pragma unroll
          for (int d = 0; d < 4; ++d) { const unsigned kk = K[i + e][d], q0 = qa[d], q1 = qb[d]; u = dot2(kk, q0, u); v = dot2(kk, q1, v); }
          t[e] = sum8(u + dpp<ROR8>(v));
        }
        const unsigned r1 = pk(t[0], t[1]);
        const unsigned r2 = pk(lo(r1) * 0.08838834764831845f, hi(r1) * 0.08838834764831845f);
        sc[i] = lo(r2); sc[i + 1] = hi(r2);
        mx = fmaxf(mx, fmaxf(sc[i], sc[i + 1]));
      }
      if constexpr (n == PV0 - 1) {
        mx = max_x32(max_x16(mx));
        mneg = -mx * 1.4426950408889634f;
#pragma unroll
        for (int i = 0; i < 32; ++i) { sc[i] = __builtin_amdgcn_exp2f(fmaf(sc[i], 1.4426950408889634f, mneg)); lsum += sc[i]; }
        lsum = sum_x32(sum_x16(lsum));
        if ((lane & 0x37) == 0) *(LAS f32x2*)(L + L_ST + w * 4 + sel * 2) = f32x2{mx, lsum};
        lds_signal(L + C_ST);
        TS(4);
      }
      if constexpr (n >= PV0) {
        constexpr int done = (n - 1 - PV0) * 2 + 2 < n - 1 - 32 - WIN + 1 ? (n - 1 - PV0) * 2 + 2 : n - 1 - 32 - WIN + 1;
        constexpr int upto = (n - PV0) * 2 + 2 < n - 32 - WIN + 1 ? (n - PV0) * 2 + 2 : n - 32 - WIN + 1;
        static_for<32>([&](auto jc) {
          constexpr int i = jc.value;
          if constexpr (i >= (n == PV0 ? 0 : done) && i < upto) acc = gemv4(pk(sc[i], sc[i]), mask, V[i], acc);
          if constexpr (i == 31 && i >= done && i < upto) {
            TS(5);
            *(LAS f32x4*)(L + L_D + w * 256 + lane * 4) = acc;
            lds_signal(L + C_D);
          }
        });
      }
      __builtin_amdgcn_sched_barrier(0);
    });
    static_assert(73 - 32 - WIN + 1 >= 32, "all V loads are consumed inside the step list");
  } else {
    __builtin_amdgcn_s_setprio(2);
    const int ww = w - 4;
    const rsrc_t rx = mk(a.x, 2 * D * 2);
    const u32x2 ev = __builtin_amdgcn_raw_buffer_load_b64(rw, WS_E, 0, AUX_SC1);
    u32x3 xs[3], x0[3], x1[3];
#pragma unroll
    for (int i = 0; i < 3; ++i) xs[i] = __builtin_amdgcn_raw_buffer_load_b96(rx, lane * 36 + i * 12, 0, 0);
    const int sl = isq ? ww : gi * 3 + ww;
    const bool act = isq || ww < 3;
    const int r0 = isq ? 144 * ww + 18 * (2 * G + sel) : 72 * (sl & 7) + 18 * G;
    const int blk = sl >> 3, mat = blk >= 36, cb = blk - 36 * mat;
#pragma unroll
    for (int i = 0; i < 3; ++i) {
      x0[i] = __builtin_amdgcn_raw_buffer_load_b96(rx, (isq ? 0 : sel * (D * 2)) + r0 * 2 + i * 12, 0, 0);
      x1[i] = __builtin_amdgcn_raw_buffer_load_b96(rx, isq ? D * 2 + r0 * 2 + i * 12 : OOB, 0, 0);
    }
    const int rstride = isq ? NQ * 2 : F * 2;
    const rsrc_t r1 = mk(isq ? a.wq : mat ? a.wu : a.wg, D * rstride);
    const int woff = !act ? OOB : isq ? r0 * rstride + qi * 128 + (lane & 7) * 16 : r0 * rstride + cb * 256 + cc * 16;
    u32x4 Wt[18];
#pragma unroll
    for (int i = 0; i < 18; ++i) Wt[i] = ld_nt(r1, woff, i * rstride);
    __builtin_amdgcn_sched_barrier(0);
    __builtin_amdgcn_s_barrier();
    CBAR();
    TS(1);
    const int ooff = WS_O + ((kvh * 16 + (lane >> 2)) * 16 + j) * 64 + (lane & 3) * 16;
    const int stoff = (lane & 31) == 0 ? WS_ST + ((kvh * 2 + (lane >> 5)) * 16 + j) * 8 : OOB;
    if (ww == 0) {
      st_sc(rw, ooff, 0, empty4);
      __builtin_amdgcn_raw_buffer_store_b64(u32x2{EMPTY, EMPTY}, rw, stoff, 0, AUX_SC1);
    } else if (ww == 1) st_sc(rw, yoff, 0, empty4);
    float sm = 0.f;
#pragma unroll
    for (int i = 0; i < 9; ++i) { const unsigned t = xs[i / 3][i % 3]; sm += lo(t) + hi(t); }
    sm = sum8(sm); sm += dpp<MIR>(sm); sm = sum_x16(sm);
    const float mean = sm * (1.f / D);
    float sv = 0.f;
#pragma unroll
    for (int i = 0; i < 9; ++i) { const unsigned t = xs[i / 3][i % 3]; const float d0 = lo(t) - mean, d1 = hi(t) - mean; sv = fmaf(d0, d0, fmaf(d1, d1, sv)); }
    sv = sum8(sv); sv += dpp<MIR>(sv); sv = sum_x16(sv);
    const float mb = rbf(mean), sd = rbf(sqrtf(rbf(rbf(sv * (1.f / D)) + 1e-5f))), rs = __builtin_amdgcn_rcpf(sd);
    const float m0 = asf(__builtin_amdgcn_readlane(asu(mb), 0)), m1 = asf(__builtin_amdgcn_readlane(asu(mb), 32));
    const float s0 = asf(__builtin_amdgcn_readlane(asu(sd), 0)), s1 = asf(__builtin_amdgcn_readlane(asu(sd), 32));
    const float i0 = asf(__builtin_amdgcn_readlane(asu(rs), 0)), i1 = asf(__builtin_amdgcn_readlane(asu(rs), 32));
    const u64 tag = (((u64)(unsigned)__builtin_amdgcn_readfirstlane(ev.y) << 32) | (unsigned)__builtin_amdgcn_readfirstlane(ev.x)) + 1;
    const unsigned tag_lo16 = (unsigned)tag << 16, tag_hi = (unsigned)(tag >> 16);
    if (isq) {
      unsigned c0[18], c1[18];
      normalize18(x0, m0, s0, i0, c0);
      normalize18(x1, m1, s1, i1, c1);
      f32x4 d0 = {0.f, 0.f, 0.f, 0.f}, d1 = d0;
#pragma unroll
      for (int i = 0; i < 18; ++i) { d0 = gemv4(c0[i], mask, Wt[i], d0); d1 = gemv4(c1[i], mask, Wt[i], d1); }
      TS(2);
      if (sel == (lane >> 5)) {
        LAS f32x4* qp = (LAS f32x4*)(L + L_QP) + ww * 64 + sel * 16 + (G & 1) * 8 + (lane & 7);
        qp[0] = d0; qp[32] = d1;
      }
      lds_signal(L + C_QP);
      if (ww == 0) {
        lds_wait(L + C_QP, 4);
        const int rr = lane >> 5, b = G & 1;
        f32x2 t = {0.f, 0.f};
#pragma unroll
        for (int i = 0; i < 8; ++i) t += *(LAS f32x2*)(L + L_QP + ((i >> 1) * 64 + b * 32 + (i & 1) * 16 + cc) * 4 + rr * 2);
        const unsigned qq = pk(t.x, t.y);
        st_sc(rw, WS_Q + (((qi >> 2) * 2 + b) * 256 + (qi & 3) * 64 + (lane & 7) * 8 + sel * 4 + rr * 2) * 8, 0,
              u32x4{(qq & 0xffffu) | tag_lo16, tag_hi, (qq >> 16) | tag_lo16, tag_hi});
      }
    } else if (act) {
      unsigned c0[18];
      normalize18(x0, sel ? m1 : m0, sel ? s1 : s0, sel ? i1 : i0, c0);
      f32x4 d0 = {0.f, 0.f, 0.f, 0.f};
#pragma unroll
      for (int i = 0; i < 18; ++i) d0 = gemv4(c0[i], mask, Wt[i], d0);
      TS(2);
      const int f4 = cb * 32 + cc * 2 + (G & 1), cons = f4 / 6;
      st_sc(rw, WS_GU + cons * 3072 + ((mat * 12 + (G >> 1) * 6 + (f4 - cons * 6)) * 8 + (sl & 7)) * 16, 0, __builtin_bit_cast(u32x4, d0));
    }
    if (ww == 3 && c < 72 && lane < 4) {
      const int sg = c >> 4, g2 = sg < 4 ? (c >> 2) & 3 : (c >> 1) & 3, mm = sg < 4 ? c & 3 : c & 1;
      *(LAS u32x2*)(L + L_XR + lane * 2) = __builtin_amdgcn_raw_buffer_load_b64(rx, ((g2 >> 1) * D + 128 * sg + 8 * (4 * mm + lane) + 4 * (g2 & 1)) * 2, 0, 0);
    }
    DRAIN_VM();
    TS(3);
    if (ww != 1) lds_signal(L + C_INIT);

    if (ww == 0) {
      u32x4 g0, g1;
      const int qoff = WS_Q + (kvh * 2 + bb) * 2048 + lane * 16;
      for (unsigned spin = 0; spin < SPIN_CAP; ++spin) {
        CBAR();
        g0 = ld_sc(rw, qoff, 0); g1 = ld_sc(rw, qoff, 1024);
        const bool ok = ((g0.x & 0xffff0000u) == tag_lo16) & (g0.y == tag_hi) & ((g0.z & 0xffff0000u) == tag_lo16) & (g0.w == tag_hi) &
                        ((g1.x & 0xffff0000u) == tag_lo16) & (g1.y == tag_hi) & ((g1.z & 0xffff0000u) == tag_lo16) & (g1.w == tag_hi);
        if (__all(ok)) break;
        __builtin_amdgcn_s_sleep(8);
      }
      L[L_Q + lane] = (g0.x & 0xffffu) | (g0.z << 16);
      L[L_Q + 64 + lane] = (g1.x & 0xffffu) | (g1.z << 16);
      lds_signal(L + F_Q);
      TS(4);
      lds_wait(L + C_ST, 4);
      f32x2 st[4];
#pragma unroll
      for (int i = 0; i < 4; ++i) st[i] = *(LAS f32x2*)(L + L_ST + i * 4 + (lane >> 5) * 2);
      const float M = fmaxf(fmaxf(st[0].x, st[1].x), fmaxf(st[2].x, st[3].x));
      float fw[4], Ls = 0.f;
#pragma unroll
      for (int i = 0; i < 4; ++i) { fw[i] = __builtin_amdgcn_exp2f((st[i].x - M) * 1.4426950408889634f); Ls = fmaf(fw[i], st[i].y, Ls); }
      __builtin_amdgcn_raw_buffer_store_b64(u32x2{min(asu(M), EMPTY - 1), min(asu(Ls), EMPTY - 1)}, rw, stoff, 0, AUX_SC1);
      lds_wait(L + F_OK, 1);
      TS(5);
      lds_wait(L + C_D, 4);
      f32x4 o = {0.f, 0.f, 0.f, 0.f};
#pragma unroll
      for (int i = 0; i < 4; ++i) o += fw[i] * *(LAS f32x4*)(L + L_D + i * 256 + lane * 4);
      st_sc(rw, ooff, 0, payload(o));
      TS(6);
      u32x4 ov; u32x2 ml;
      const int goff = WS_O + ((kvh * 16 + j) * 16 + (lane & 15)) * 64 + G * 16, moff = WS_ST + ((kvh * 2 + (j >> 3)) * 16 + (lane & 15)) * 8;
      __builtin_amdgcn_s_sleep(4);
      for (unsigned spin = 0; spin < SPIN_CAP; ++spin) {
        CBAR();
        ov = ld_sc(rw, goff, 0);
        ml = __builtin_amdgcn_raw_buffer_load_b64(rw, moff, 0, AUX_SC1);
        if (__all(filled(ov) & (max(ml.x, ml.y) != EMPTY))) break;
      }
      const float e = __builtin_amdgcn_exp2f((asf(ml.x) - max8(asf(ml.x))) * 1.4426950408889634f);
      const float f = e * __builtin_amdgcn_rcpf(sum8(e * asf(ml.y)));
      const float o0 = sum8(f * asf(ov.x)), o1 = sum8(f * asf(ov.y)), o2 = sum8(f * asf(ov.z)), o3 = sum8(f * asf(ov.w));
      if ((lane & 7) == 0) *(LAS u32x4*)(L + L_CO + (lane >> 3) * 4) = u32x4{pk(o0, o0), pk(o1, o1), pk(o2, o2), pk(o3, o3)};
      lds_signal(L + F_O);
      TS(7);
    } else if (ww == 1) {
      lds_wait(L + C_INIT, 7);
      if (lane == 0) __builtin_amdgcn_raw_buffer_store_b64(u32x2{(unsigned)tag, (unsigned)(tag >> 32)}, rw, WS_FLAG + c * 8, 0, AUX_SC1);
      lds_wait(L + F_Q, 1);
      for (unsigned spin = 0; spin < SPIN_CAP; ++spin) {
        __builtin_amdgcn_s_sleep(16);
        CBAR();
        const u32x4 f0 = ld_sc(rw, WS_FLAG + lane * 16, 0), f1 = ld_sc(rw, WS_FLAG + lane * 16, 1024);
        const unsigned tl = (unsigned)tag, th = (unsigned)(tag >> 32);
        if (__all((f0.x == tl) & (f0.y == th) & (f0.z == tl) & (f0.w == th) & (f1.x == tl) & (f1.y == th) & (f1.z == tl) & (f1.w == th))) break;
      }
      CBAR();
      lds_signal(L + F_OK);
      TS(4);
      if (c == 0 && lane == 0)
        __builtin_amdgcn_raw_buffer_store_b64(u32x2{(unsigned)tag, (unsigned)(tag >> 32)}, rw, WS_E, 0, AUX_SC1);
      if (!isq) {
        u32x4 p[3];
#pragma unroll
        for (int i = 0; i < 3; ++i) p[i] = ld_sc(rw, WS_GU + gi * 3072 + lane * 16, i * 1024);
#pragma unroll
        for (int i = 0; i < 3; ++i) {
          const f32x4 t = {sum8(asf(p[i].x)), sum8(asf(p[i].y)), sum8(asf(p[i].z)), sum8(asf(p[i].w))};
          if ((lane & 7) == 0) *(LAS f32x4*)(L + L_GU + (i * 8 + (lane >> 3)) * 4) = t;
        }
        __builtin_amdgcn_fence(__ATOMIC_ACQ_REL, "wavefront", "local");
        if (lane < 48) { const float h = rbf(asf(L[L_GU + lane])) * rbf(asf(L[L_GU + 48 + lane])); L[L_CH + lane] = pk(h, h); }
      }
      lds_signal(L + F_H);
      TS(5);
      lds_wait(L + F_V, 1);
#pragma unroll
      for (int i = 0; i < 4; ++i) W3[i] = ld_nt(ro, wo_off, i * (D * 2));
#pragma unroll
      for (int i = 0; i < 6; ++i) W3[4 + i] = ld_nt(rd, wd_off, i * (D * 2));
      __builtin_amdgcn_sched_barrier(0);
    }
  }

  if (w < 4 || w == 5) {
    if (!isq) {
      lds_wait(L + F_H, 1);
      u32x2 ch[3];
#pragma unroll
      for (int i = 0; i < 3; ++i) ch[i] = *(LAS u32x2*)(L + L_CH + sel * 24 + G * 6 + i * 2);
#pragma unroll
      for (int i = 0; i < 6; ++i) { const unsigned t = ch[i >> 1][i & 1]; y = gemv4(t, mask, W3[4 + i], y); }
    }
    TS(6);
    lds_wait(L + F_O, 1);
    const u32x4 co = *(LAS u32x4*)(L + L_CO + (lane >> 3) * 4);
    TS(7);
#pragma unroll
    for (int i = 0; i < 4; ++i) { const unsigned t = co[i]; y = gemv4(t, mask, W3[i], y); }
    st_sc(rw, yoff, 0, payload(y));
    TS(8);
  } else if (c < 72) lds_wait(L + F_O, 1);

  if (c < 72) {
    __builtin_amdgcn_s_sleep(FIN_DELAY);
    u32x4 p0, p1;
    const int off = WS_Y + c * 16384 + w * 2048 + lane * 16;
    for (unsigned spin = 0; spin < SPIN_CAP; ++spin) {
      CBAR();
      p0 = ld_sc(rw, off, 0); p1 = ld_sc(rw, off, 1024);
      if (__all(filled(p0) & filled(p1))) break;
    }
    TS(9);
    f32x4 t = __builtin_bit_cast(f32x4, p0) + __builtin_bit_cast(f32x4, p1);
#pragma unroll
    for (int i = 0; i < 4; ++i) { float u = t[i]; u += dpp<ROR4>(u); u += dpp<ROR8>(u); t[i] = sum_x32(sum_x16(u)); }
    if (lane < 4) *(LAS f32x4*)(L + L_FIN + w * 16 + lane * 4) = t;
    __builtin_amdgcn_fence(__ATOMIC_RELEASE, "workgroup", "local");
    unsigned old = 0;
    if (lane == 0) old = __hip_atomic_fetch_add(L + C_FIN, 1u, __ATOMIC_RELAXED, __HIP_MEMORY_SCOPE_WORKGROUP);
    if (__builtin_amdgcn_readfirstlane(old) == 7) {
      __builtin_amdgcn_fence(__ATOMIC_ACQUIRE, "workgroup", "local");
      if (lane < 4) {
        f32x4 r = *(LAS f32x4*)(L + L_FIN + lane * 4);
#pragma unroll
        for (int i = 1; i < 8; ++i) r += *(LAS f32x4*)(L + L_FIN + i * 16 + lane * 4);
        const u32x2 xr = *(LAS u32x2*)(L + L_XR + lane * 2);
        const int sg = c >> 4, g2 = sg < 4 ? (c >> 2) & 3 : (c >> 1) & 3, mm = sg < 4 ? c & 3 : c & 1;
        *(u32x2*)(a.out + (g2 >> 1) * D + 128 * sg + 8 * (4 * mm + lane) + 4 * (g2 & 1)) =
            u32x2{pk(lo(xr.x) + r.x, hi(xr.x) + r.y), pk(lo(xr.y) + r.z, hi(xr.y) + r.w)};
      }
    }
    TS(10);
  }
  TSFLUSH();
}

}

namespace probe {
typedef unsigned u32x4 __attribute__((ext_vector_type(4)));
template <int I> struct IC { static constexpr int value = I; };
template <int N, int I = 0, class F> __device__ __forceinline__ void sfor(F&& f) { if constexpr (I < N) { f(IC<I>{}); sfor<N, I + 1>(f); } }
#define RS(p, n) __builtin_amdgcn_make_buffer_rsrc((void*)(p), 0, (n), 0x00020000)
__global__ void k_empty(unsigned* ws) {}
template <int AUX>
__global__ void __launch_bounds__(256) k_stream(const char* k, const char* v, unsigned* ws) {
  const int tid = threadIdx.x, c = blockIdx.x;
  auto rk = RS(k + (size_t)c * 131072, 131072), rv = RS(v + (size_t)c * 131072, 131072);
  u32x4 a[8]; u32x4 acc = {0, 0, 0, 0};
  auto load = [&](auto ic) -> u32x4 { constexpr int i = ic.value;
    if constexpr (i < 32) return __builtin_amdgcn_raw_buffer_load_b128(rk, tid * 16, i * 4096, AUX);
    else return __builtin_amdgcn_raw_buffer_load_b128(rv, tid * 16, (i - 32) * 4096, AUX); };
  sfor<8>([&](auto ic) { a[ic.value] = load(ic); });
  __builtin_amdgcn_sched_barrier(0);
  sfor<64>([&](auto ic) { constexpr int i = ic.value; acc ^= a[i % 8]; asm volatile("" : "+v"(acc));
    if constexpr (i + 8 < 64) a[i % 8] = load(IC<i + 8>{}); __builtin_amdgcn_sched_barrier(0); });
  if ((acc.x ^ acc.y ^ acc.z ^ acc.w) == 0x12345678u) ws[(8 << 20) / 4] = 1;
}
#define R4(x) x x x x
#define R16(x) R4(R4(x))
#define R256(x) R16(R16(x))
__global__ void __launch_bounds__(64) k_code(unsigned* ws) {
  unsigned v = threadIdx.x;
  asm volatile(R16(R256("v_add_u32 %0, 1, %0\n\t")) : "+v"(v));
  if (v == 12345) ws[(8 << 20) / 4] = 2;
}
__global__ void __launch_bounds__(64) k_loop(unsigned* ws) {
  unsigned v = threadIdx.x;
#pragma unroll 1
  for (int i = 0; i < 64; ++i) asm volatile(R4(R16("v_add_u32 %0, 1, %0\n\t")) : "+v"(v));
  if (v == 12345) ws[(8 << 20) / 4] = 2;
}
struct TP { const char* p[10]; };
__global__ void __launch_bounds__(64) k_touch(TP t, unsigned* ws) {
  const int l = threadIdx.x;
  unsigned acc = 0;
  unsigned v[6];
  v[0] = __builtin_nontemporal_load((const unsigned*)(t.p[1] + (size_t)(l & 31) * 1048576 + blockIdx.x * 128));
  v[1] = __builtin_nontemporal_load((const unsigned*)(t.p[2] + (size_t)(l & 31) * 1048576 + blockIdx.x * 128));
  const int b = l >> 3, o = l & 7;
  const char* q = b == 0 ? t.p[3] : b == 1 ? t.p[4] : b == 2 ? t.p[6] : b == 3 ? t.p[7] : b == 4 ? t.p[8] : b == 5 ? t.p[9] : b == 6 ? t.p[0] : t.p[5];
  v[2] = __builtin_nontemporal_load((const unsigned*)(q + (b < 6 ? (size_t)(o > 4 ? 4 : o) * 1048576 + blockIdx.x * 128 : 0)));
  acc = v[0] ^ v[1] ^ v[2];
  if (acc == 0x12345) ws[(8 << 20) / 4] = 2;
}
template <int STRIDE>
__global__ void __launch_bounds__(64) k_chase(const char* k, unsigned* ws) {
  auto rk = RS(k, 33554432);
  unsigned off = blockIdx.x * 128;
#pragma unroll 1
  for (int i = 0; i < 24; ++i) { unsigned t = __builtin_amdgcn_raw_buffer_load_b32(rk, off, 0, 2); off = (off + STRIDE + __builtin_amdgcn_readfirstlane(t & 0 ) ) & 33554431; asm volatile("" : "+v"(off) : "v"(t)); }
  if (off == 12345) ws[(8 << 20) / 4] = 2;
}
}

extern "C" void kernel_launch(void* const* d_in, const int* in_sizes, int n_in, void* d_out, int out_size, void* d_ws, size_t ws_size,
                              hipStream_t stream) {
  Args a;
  a.x = (const u16*)d_in[0]; a.k = (const u16*)d_in[1]; a.v = (const u16*)d_in[2]; a.wq = (const u16*)d_in[3]; a.wo = (const u16*)d_in[4];
  a.wg = (const u16*)d_in[6]; a.wu = (const u16*)d_in[7]; a.wd = (const u16*)d_in[8];
  a.out = (u16*)d_out; a.ws = (unsigned char*)d_ws;
  probe::k_empty<<<1, 64, 0, stream>>>((unsigned*)d_ws);
  fused_decode<<<NWG, NTHR, 0, stream>>>(a);
}
```

```cpp
#include <hip/hip_runtime.h>
#include <hip/hip_bf16.h>
#include <stdint.h>

namespace {

typedef unsigned short u16;
typedef unsigned long long u64;
typedef unsigned u32x2 __attribute__((ext_vector_type(2)));
typedef unsigned u32x3 __attribute__((ext_vector_type(3)));
typedef unsigned u32x4 __attribute__((ext_vector_type(4)));
typedef float f32x2 __attribute__((ext_vector_type(2)));
typedef float f32x4 __attribute__((ext_vector_type(4)));
typedef __bf16 bf16x2 __attribute__((ext_vector_type(2)));
typedef __bf16 bf16x8 __attribute__((ext_vector_type(8)));
typedef __amdgpu_buffer_rsrc_t rsrc_t;
#define LAS __attribute__((address_space(3)))

constexpr int D = 576, SK = 4096, DH = 128, F = 4608, NQ = 4096;
constexpr int NWG = 256, NTHR = 512;
constexpr int AUX_NT = 2, AUX_SC1 = 16;
constexpr int OOB = 0x7ffffff0;
constexpr unsigned TM = 0x3FFu;
constexpr unsigned SPIN_CAP = 1u << 16;
#define ABL 0
#define WIN 6
#define QK0 36
#define QKR 2
#define FV_STEP 60
#define PQ_STEP 22
#define PG_STEP 40
#define CDS 0
#define OSLEEP 0
#define FOS 0
#define PRIO2 2
#define QSLEEP 16
#define FIN_DELAY 4

constexpr int WS_E = 0;
constexpr int WS_ST = 4096;
constexpr int WS_Q = 16384;
constexpr int WS_O = WS_Q + 131072;
constexpr int WS_GU = WS_O + 262144;
constexpr int WS_Y = WS_GU + 192 * 3072;
constexpr int WS_END = WS_Y + 72 * 16384;

constexpr int L_CNT = 0;
enum { C_QP = 0, F_Q, C_ST, C_D, F_H, F_O, F_V, C_FIN, TAG_LO, TAG_HI, N_CNT = 8 };
constexpr int L_Q = 16;
constexpr int L_ST = L_Q + 128;
constexpr int L_CH = L_ST + 16;
constexpr int L_CO = L_CH + 48;
constexpr int L_XR = L_CO + 32;
constexpr int L_GU = L_XR + 8;
constexpr int L_FIN = L_GU + 96;
constexpr int L_D = L_FIN + 128;
constexpr int L_QP = L_D + 1024;
constexpr int L_END = L_QP + 1024;

struct Args { const u16 *x, *k, *v, *wq, *wo, *wg, *wu, *wd; u16* out; unsigned char* ws; };

template <int I> struct IC { static constexpr int value = I; };
template <int N, int I = 0, class Fn> __device__ __forceinline__ void static_for(Fn&& f) {
  if constexpr (I < N) { f(IC<I>{}); static_for<N, I + 1>(f); }
}
__device__ __forceinline__ rsrc_t mk(const void* p, int bytes) { return __builtin_amdgcn_make_buffer_rsrc((void*)p, 0, bytes, 0x00020000); }
__device__ __forceinline__ u32x4 ld_nt(rsrc_t r, int voff, int soff) { return __builtin_amdgcn_raw_buffer_load_b128(r, voff, soff, AUX_NT); }
__device__ __forceinline__ u32x4 ld_sc(rsrc_t r, int voff, int soff) { return __builtin_amdgcn_raw_buffer_load_b128(r, voff, soff, AUX_SC1); }
__device__ __forceinline__ void st_sc(rsrc_t r, int voff, int soff, u32x4 v) { __builtin_amdgcn_raw_buffer_store_b128(v, r, voff, soff, AUX_SC1); }
__device__ __forceinline__ float asf(unsigned u) { return __builtin_bit_cast(float, u); }
__device__ __forceinline__ unsigned asu(float f) { return __builtin_bit_cast(unsigned, f); }
__device__ __forceinline__ float lo(unsigned v) { return asf(v << 16); }
__device__ __forceinline__ float hi(unsigned v) { return asf(v & 0xffff0000u); }
__device__ __forceinline__ unsigned pk(float a, float b) { const f32x2 v = {a, b}; return __builtin_bit_cast(unsigned, __builtin_convertvector(v, bf16x2)); }
__device__ __forceinline__ float rbf(float a) { return lo(pk(a, a)); }
__device__ __forceinline__ float dot2(unsigned a, unsigned b, float c) {
  return __builtin_amdgcn_fdot2_f32_bf16(__builtin_bit_cast(bf16x2, a), __builtin_bit_cast(bf16x2, b), c, false);
}
template <int CTRL> __device__ __forceinline__ float dpp(float x) {
  return asf(__builtin_amdgcn_update_dpp(0, (int)asu(x), CTRL, 0xf, 0xf, true));
}
constexpr int XOR1 = 0xB1, XOR2 = 0x4E, HMIR = 0x141, MIR = 0x140, ROR4 = 0x124, ROR8 = 0x128;
__device__ __forceinline__ float sum8(float a) { a += dpp<XOR1>(a); a += dpp<XOR2>(a); return a + dpp<HMIR>(a); }
__device__ __forceinline__ float max8(float a) { a = fmaxf(a, dpp<XOR1>(a)); a = fmaxf(a, dpp<XOR2>(a)); return fmaxf(a, dpp<HMIR>(a)); }
__device__ __forceinline__ float sum_x16(float a) { auto s = __builtin_amdgcn_permlane16_swap(asu(a), asu(a), false, false); return asf(s[0]) + asf(s[1]); }
__device__ __forceinline__ float sum_x32(float a) { auto s = __builtin_amdgcn_permlane32_swap(asu(a), asu(a), false, false); return asf(s[0]) + asf(s[1]); }
__device__ __forceinline__ float max_x16(float a) { auto s = __builtin_amdgcn_permlane16_swap(asu(a), asu(a), false, false); return fmaxf(asf(s[0]), asf(s[1])); }
__device__ __forceinline__ float max_x32(float a) { auto s = __builtin_amdgcn_permlane32_swap(asu(a), asu(a), false, false); return fmaxf(asf(s[0]), asf(s[1])); }
__device__ __forceinline__ f32x4 gemv4(unsigned coef2, const u32x4& mask, const u32x4& w, f32x4 acc) {
  const u32x4 a = {coef2 & mask.x, coef2 & mask.y, coef2 & mask.z, coef2 & mask.w};
  return __builtin_amdgcn_mfma_f32_16x16x32_bf16(__builtin_bit_cast(bf16x8, a), __builtin_bit_cast(bf16x8, w), acc, 0, 0, 0);
}
__device__ __forceinline__ unsigned enc(float v, unsigned t10) { return ((asu(v) + 0x200u) & ~TM) | t10; }
__device__ __forceinline__ u32x4 enc4(f32x4 v, unsigned t10) { return u32x4{enc(v.x, t10), enc(v.y, t10), enc(v.z, t10), enc(v.w, t10)}; }
__device__ __forceinline__ f32x4 dec4(const u32x4& v) { return f32x4{asf(v.x & ~TM), asf(v.y & ~TM), asf(v.z & ~TM), asf(v.w & ~TM)}; }
__device__ __forceinline__ unsigned diff4(const u32x4& v, unsigned t) { return ((v.x ^ t) | (v.y ^ t)) | ((v.z ^ t) | (v.w ^ t)); }
#define CBAR() asm volatile("" ::: "memory")
#define DRAIN_VM() asm volatile("s_waitcnt vmcnt(0)" ::: "memory")
template <int N> __device__ __forceinline__ void throttle() { asm volatile("s_waitcnt vmcnt(%0)" ::"i"(N) : "memory"); }
__device__ __forceinline__ void lds_signal(LAS unsigned* p) {
  __builtin_amdgcn_fence(__ATOMIC_RELEASE, "workgroup", "local");
  if ((threadIdx.x & 63) == 0) __hip_atomic_fetch_add(p, 1u, __ATOMIC_RELAXED, __HIP_MEMORY_SCOPE_WORKGROUP);
  CBAR();
}
template <int SLEEP> __device__ __forceinline__ void lds_wait(LAS unsigned* p, unsigned n) {
  while ((unsigned)__builtin_amdgcn_readfirstlane(__hip_atomic_load(p, __ATOMIC_RELAXED, __HIP_MEMORY_SCOPE_WORKGROUP)) < n)
    if (SLEEP) __builtin_amdgcn_s_sleep(SLEEP);
  __builtin_amdgcn_fence(__ATOMIC_ACQUIRE, "workgroup", "local");
}
#define TS(i)
#define TSDECL
#define TSFLUSH()

__device__ __forceinline__ void normalize18(const u32x3 (&r)[3], float m, float sd, float rs, unsigned (&o)[18]) {
#pragma unroll
  for (int i = 0; i < 9; ++i) {
    const unsigned t = r[i / 3][i % 3];
    const unsigned d = pk(lo(t) - m, hi(t) - m);
    const float d0 = lo(d), d1 = hi(d);
    float q0 = d0 * rs, q1 = d1 * rs;
    q0 = fmaf(fmaf(-q0, sd, d0), rs, q0);
    q1 = fmaf(fmaf(-q1, sd, d1), rs, q1);
    o[2 * i] = pk(q0, q0);
    o[2 * i + 1] = pk(q1, q1);
  }
}

__global__ void __launch_bounds__(NTHR, 2) fused_decode(Args a) {
  __shared__ __attribute__((aligned(16))) unsigned lds_raw[L_END];
  LAS unsigned* L = (LAS unsigned*)lds_raw;
  TSDECL;
  const u64 pad_t0 = __builtin_amdgcn_s_memrealtime();
  TS(0);
  const int lane = threadIdx.x & 63, w = __builtin_amdgcn_readfirstlane(threadIdx.x >> 6);
  const int c = blockIdx.x, s = (c >> 3) & 7, pair = (c & 7) * 4 + (c >> 6), bb = pair & 1, kvh = pair >> 1;
  const bool isq = (s & 3) == 0;
  const int qi = pair * 2 + (s >> 2), gi = pair * 6 + (s >> 2) * 3 + (s & 3) - 1;
  const int G = lane >> 4, cc = lane & 15, sel = (lane >> 3) & 1;
  const rsrc_t rw = mk(a.ws, WS_END);
  const u32x4 mask = {(lane & 7) == 0 ? 0xffffu : (lane & 7) == 1 ? 0xffff0000u : 0u, (lane & 7) == 2 ? 0xffffu : (lane & 7) == 3 ? 0xffff0000u : 0u,
                      (lane & 7) == 4 ? 0xffffu : (lane & 7) == 5 ? 0xffff0000u : 0u, (lane & 7) == 6 ? 0xffffu : (lane & 7) == 7 ? 0xffff0000u : 0u};
  const int j = bb * 8 + s;
  const int seg = w < 4 ? w : 4;
  const bool segok = seg < 4 || cc < 8;
  const int yoff = segok ? WS_Y + ((seg * 16 + (seg < 4 ? (lane >> 2) : G * 2 + (cc >> 2))) * 256 + c) * 64 + (lane & 3) * 16 : OOB;
  const int wo_off = segok ? (kvh * 256 + (j >> 3) * 128 + 8 * (4 * (j & 3) + G) + 4 * ((j >> 2) & 1)) * (D * 2) + seg * 256 + cc * 16 : OOB;
  const int wd_off = segok && !isq ? G * (D * 2) + seg * 256 + cc * 16 : OOB;
  auto wd_row = [&](int i) { const int li = gi * 6 + i; return (128 * (li >> 5) + 8 * (li & 15) + 4 * ((li >> 4) & 1)) * (D * 2); };
  const rsrc_t ro = mk(a.wo, NQ * D * 2), rd = mk(a.wd, F * D * 2);
  u32x4 W3[10];
  f32x4 y = {0.f, 0.f, 0.f, 0.f};

  if (w < 4) {
    if (w == 0 && lane < N_CNT) L[L_CNT + lane] = 0u;
    const size_t kvbase = ((size_t)(bb * 16 + kvh) * SK + s * 512) * DH;
    const rsrc_t rk = mk(a.k + kvbase, 512 * DH * 2), rv = mk(a.v + kvbase, 512 * DH * 2);
    const int voff = w * 1024 + lane * 16;
    asm volatile("s_waitcnt lgkmcnt(0)" ::: "memory");
    __builtin_amdgcn_s_barrier();
    CBAR();
    TS(1);
    u32x4 K[32], V[32], qa, qb;
    float sc[8];
    unsigned pp[8];
    f32x4 acc = {0.f, 0.f, 0.f, 0.f};
    float mx = -INFINITY, lsum = 0.f;
    constexpr int PV0 = QK0 + 32 / QKR;
    static_for<74>([&](auto ic) {
      constexpr int n = ic.value;
      if constexpr (n >= WIN) throttle<WIN - 1>();
      if constexpr (n < 32) K[n] = ld_nt(rk, voff, n * 4096);
      else if constexpr (n < 64) V[n - 32] = ld_nt(rv, voff, (n - 32) * 4096);
      else if constexpr (n < 68) W3[n - 64] = ld_nt(ro, wo_off, (n - 64) * (D * 2));
      else W3[n - 64] = ld_nt(rd, wd_off, wd_row(n - 68));
      if constexpr (n == PQ_STEP || n == PG_STEP || n == FV_STEP) { if (w == 0) lds_signal(L + F_V); }
      if constexpr (n == QK0) {
        TS(2);
        if (!(ABL & 4)) lds_wait<1>(L + F_Q, 1);
        qa = *(LAS u32x4*)(L + L_Q + sel * 64 + cc * 4);
        qb = *(LAS u32x4*)(L + L_Q + (sel ^ 1) * 64 + cc * 4);
        TS(3);
      }
      if constexpr (n >= QK0 && n < PV0 && (n - QK0) % (4 / QKR) == 0) {
        constexpr int g = (n - QK0) * QKR / 4;
        float t[4];
#pragma unroll
        for (int e = 0; e < 4; ++e) {
          float u = 0.f, v = 0.f;
#pragma unroll
          for (int d = 0; d < 4; ++d) { const unsigned kk = K[4 * g + e][d], q0 = qa[d], q1 = qb[d]; u = dot2(kk, q0, u); v = dot2(kk, q1, v); }
          t[e] = u + dpp<ROR8>(v);
          asm volatile("" : "+v"(t[e]));
          t[e] += dpp<HMIR>(t[e]);
          asm volatile("" : "+v"(t[e]));
        }
        const bool o1 = lane & 1, o2 = lane & 2;
        float r0 = (o1 ? t[1] : t[0]) + dpp<XOR1>(o1 ? t[0] : t[1]);
        asm volatile("" : "+v"(r0));
        float r1 = (o1 ? t[3] : t[2]) + dpp<XOR1>(o1 ? t[2] : t[3]);
        const float r = (o2 ? r1 : r0) + dpp<XOR2>(o2 ? r0 : r1);
        sc[g] = rbf(rbf(r) * 0.08838834764831845f);
        mx = fmaxf(mx, sc[g]);
      }
      if constexpr (n == PV0 - 1) {
        mx = fmaxf(mx, dpp<XOR1>(mx)); mx = fmaxf(mx, dpp<XOR2>(mx));
        mx = max_x32(max_x16(mx));
        const float mneg = -mx * 1.4426950408889634f;
#pragma unroll
        for (int i = 0; i < 8; ++i) { const float e = __builtin_amdgcn_exp2f(fmaf(sc[i], 1.4426950408889634f, mneg)); lsum += e; pp[i] = pk(e, e); }
        lsum += dpp<XOR1>(lsum); lsum += dpp<XOR2>(lsum);
        lsum = sum_x32(sum_x16(lsum));
        if ((lane & 0x37) == 0) *(LAS f32x2*)(L + L_ST + w * 4 + sel * 2) = f32x2{mx, lsum};
        lds_signal(L + C_ST);
        TS(4);
      }
      if constexpr (n >= PV0) {
        constexpr int done = (n - PV0) * 4 < n - 32 - WIN ? (n - PV0) * 4 : n - 32 - WIN;
        constexpr int upto = (n - PV0 + 1) * 4 < n - 32 - WIN + 1 ? (n - PV0 + 1) * 4 : n - 32 - WIN + 1;
        static_for<32>([&](auto jc) {
          constexpr int i = jc.value;
          if constexpr (i >= done && i < upto) acc = gemv4(__builtin_amdgcn_update_dpp(0u, pp[i >> 2], (i & 3) * 0x55, 0xf, 0xf, true), mask, V[i], acc);
          if constexpr (i == 31 && i >= done && i < upto) {
            TS(5);
            *(LAS f32x4*)(L + L_D + w * 256 + lane * 4) = acc;
            lds_signal(L + C_D);
          }
        });
      }
      __builtin_amdgcn_sched_barrier(0);
    });
    static_assert(73 - 32 - WIN + 1 >= 32, "all V loads are consumed inside the step list");
  } else {
    __builtin_amdgcn_s_setprio(2);
    const int ww = w - 4;
    const rsrc_t rx = mk(a.x, 2 * D * 2);
    const u32x2 ev = __builtin_amdgcn_raw_buffer_load_b64(rw, WS_E, 0, AUX_SC1);
    u32x3 xs[3], x0[3];
#pragma unroll
    for (int i = 0; i < 3; ++i) xs[i] = __builtin_amdgcn_raw_buffer_load_b96(rx, lane * 36 + i * 12, 0, 0);
    const int sl = isq ? qi * 4 + ww : gi * 3 + ww;
    const bool act = isq || ww < 3;
    const int r0 = 72 * (sl & 7) + 18 * G;
    const int blk = sl >> 3, mat = blk >= 36, cb = blk - 36 * mat;
#pragma unroll
    for (int i = 0; i < 3; ++i) x0[i] = __builtin_amdgcn_raw_buffer_load_b96(rx, sel * (D * 2) + r0 * 2 + i * 12, 0, 0);
    const int rstride = isq ? NQ * 2 : F * 2;
    const rsrc_t r1 = mk(isq ? a.wq : mat ? a.wu : a.wg, D * rstride);
    const int woff = act ? r0 * rstride + cb * 256 + cc * 16 : OOB;
    u32x4 Wt[18];
#pragma unroll
    for (int i = 0; i < 18; ++i) { Wt[i] = ld_nt(r1, woff, i * rstride); __builtin_amdgcn_sched_barrier(0); }
    __builtin_amdgcn_s_barrier();
    CBAR();
    TS(1);
    float sm = 0.f;
#pragma unroll
    for (int i = 0; i < 9; ++i) { const unsigned t = xs[i / 3][i % 3]; sm += lo(t) + hi(t); }
    sm = sum8(sm); sm += dpp<MIR>(sm); sm = sum_x16(sm);
    const float mean = sm * (1.f / D);
    float sv = 0.f;
#pragma unroll
    for (int i = 0; i < 9; ++i) { const unsigned t = xs[i / 3][i % 3]; const float d0 = lo(t) - mean, d1 = hi(t) - mean; sv = fmaf(d0, d0, fmaf(d1, d1, sv)); }
    sv = sum8(sv); sv += dpp<MIR>(sv); sv = sum_x16(sv);
    const float mb = rbf(mean), sd = rbf(sqrtf(rbf(rbf(sv * (1.f / D)) + 1e-5f))), rs = __builtin_amdgcn_rcpf(sd);
    const float m0 = asf(__builtin_amdgcn_readlane(asu(mb), 0)), m1 = asf(__builtin_amdgcn_readlane(asu(mb), 32));
    const float s0 = asf(__builtin_amdgcn_readlane(asu(sd), 0)), s1 = asf(__builtin_amdgcn_readlane(asu(sd), 32));
    const float i0 = asf(__builtin_amdgcn_readlane(asu(rs), 0)), i1 = asf(__builtin_amdgcn_readlane(asu(rs), 32));
    const u64 tag = (((u64)(unsigned)__builtin_amdgcn_readfirstlane(ev.y) << 32) | (unsigned)__builtin_amdgcn_readfirstlane(ev.x)) + 1;
    const unsigned tlo = (unsigned)tag, thi = (unsigned)(tag >> 32), t16 = tlo & 0xffffu, t10 = tlo & TM;
    if (act) {
      unsigned c0[18];
      normalize18(x0, sel ? m1 : m0, sel ? s1 : s0, sel ? i1 : i0, c0);
      f32x4 d0 = {0.f, 0.f, 0.f, 0.f};
#pragma unroll
      for (int i = 0; i < 18; ++i) d0 = gemv4(c0[i], mask, Wt[i], d0);
      TS(2);
      if (isq) {
        *(LAS f32x4*)(L + L_QP + ww * 256 + lane * 4) = d0;
        lds_signal(L + C_QP);
        if (ww == 0) {
          lds_wait<0>(L + C_QP, 4);
#pragma unroll
          for (int i = 1; i < 4; ++i) d0 += *(LAS f32x4*)(L + L_QP + i * 256 + lane * 4);
          const int qo = WS_Q + ((((qi >> 2) * 2 + (lane >> 5)) * 4 + (qi & 3)) * 64 + (lane & 31)) * 16;
          st_sc(rw, qo, 0, u32x4{asu(d0.x), asu(d0.y), tlo, thi});
          st_sc(rw, qo, 512, u32x4{asu(d0.z), asu(d0.w), tlo, thi});
        }
      } else if (!(ABL & 2)) {
        const int li = cb * 32 + (lane & 31), cons = li / 6;
        st_sc(rw, WS_GU + cons * 3072 + (((mat * 8 + (sl & 7)) * 2 + (lane >> 5)) * 6 + (li - cons * 6)) * 16, 0, enc4(d0, t10));
      } else asm volatile("" :: "v"(d0));
    }
    if (ww == 3 && c < 72 && lane < 4) {
      const int sg = c >> 4, g2 = sg < 4 ? (c >> 2) & 3 : (c >> 1) & 3, mm = sg < 4 ? c & 3 : c & 1;
      *(LAS u32x2*)(L + L_XR + lane * 2) = __builtin_amdgcn_raw_buffer_load_b64(rx, ((g2 >> 1) * D + 128 * sg + 8 * (4 * mm + lane) + 4 * (g2 & 1)) * 2, 0, 0);
    }
    if (PRIO2 != 2) __builtin_amdgcn_s_setprio(PRIO2);
    TS(3);
    if (ABL & 4) { TSFLUSH(); return; }
    if (ww == 0) {
      lds_wait<4>(L + F_V, 1);
      u32x4 g[4];
      const int qoff = WS_Q + (kvh * 2 + bb) * 4096 + lane * 16;
#pragma unroll 1
      for (unsigned spin = 0; spin < SPIN_CAP; ++spin) {
        CBAR();
        unsigned bad = 0;
        if (ABL & 16) { for (int i = 0; i < 4; ++i) g[i] = u32x4{tlo, tlo, tlo, tlo}; break; }
#pragma unroll
        for (int i = 0; i < 4; ++i) g[i] = ld_sc(rw, qoff, i * 1024);
#pragma unroll
        for (int i = 0; i < 4; ++i) bad |= (g[i].z ^ tlo) | (g[i].w ^ thi);
        if (__all(bad == 0)) break;
        __builtin_amdgcn_s_sleep(QSLEEP);
      }
#pragma unroll
      for (int h = 0; h < 2; ++h)
        L[L_Q + h * 64 + cc * 4 + (G & 1) * 2 + (lane >> 5)] = pk(asf(g[2 * h].x) + asf(g[2 * h + 1].x), asf(g[2 * h].y) + asf(g[2 * h + 1].y));
      if (lane == 0) *(LAS u32x2*)(L + TAG_LO) = u32x2{tlo, thi};
      lds_signal(L + F_Q);
      TS(4);
      if (ABL & 64) { TSFLUSH(); return; }
      const int ooff = WS_O + ((kvh * 16 + (lane >> 2)) * 16 + j) * 64 + (lane & 3) * 16;
      const int goff = WS_O + ((kvh * 16 + j) * 16 + cc) * 64 + G * 16, moff = WS_ST + ((kvh * 2 + (j >> 3)) * 16 + cc) * 16;
      lds_wait<4>(L + C_ST, 4);
      f32x2 st[4];
#pragma unroll
      for (int i = 0; i < 4; ++i) st[i] = *(LAS f32x2*)(L + L_ST + i * 4 + (lane >> 5) * 2);
      const float M = fmaxf(fmaxf(st[0].x, st[1].x), fmaxf(st[2].x, st[3].x));
      float fw[4], Ls = 0.f;
#pragma unroll
      for (int i = 0; i < 4; ++i) { fw[i] = __builtin_amdgcn_exp2f((st[i].x - M) * 1.4426950408889634f); Ls = fmaf(fw[i], st[i].y, Ls); }
      st_sc(rw, (lane & 31) == 0 ? WS_ST + ((kvh * 2 + (lane >> 5)) * 16 + j) * 16 : OOB, 0,
            u32x4{(asu(M) & 0xffff0000u) | t16, enc(Ls, t10), tlo, thi});
      TS(5);
      lds_wait<4>(L + F_V, 3);
      lds_wait<CDS>(L + C_D, 4);
      f32x4 o = {0.f, 0.f, 0.f, 0.f};
#pragma unroll
      for (int i = 0; i < 4; ++i) o += fw[i] * *(LAS f32x4*)(L + L_D + i * 256 + lane * 4);
      st_sc(rw, ooff, 0, enc4(o, t10));
      TS(6);
      if (ABL & 128) { TSFLUSH(); return; }
      u32x4 ov, ml;
#pragma unroll 1
      for (unsigned spin = 0; spin < SPIN_CAP; ++spin) {
        CBAR();
        ml = ld_sc(rw, moff, 0);
        ov = ld_sc(rw, goff, 0);
        if (__all((((diff4(ov, t10) | (ml.y ^ t10)) & TM) | ((ml.x ^ t16) & 0xffffu) | (ml.z ^ tlo) | (ml.w ^ thi)) == 0)) break;
        if (OSLEEP) __builtin_amdgcn_s_sleep(OSLEEP);
      }
      const float ms = hi(ml.x), e = __builtin_amdgcn_exp2f((ms - max8(ms)) * 1.4426950408889634f);
      const float f = e * __builtin_amdgcn_rcpf(sum8(e * asf(ml.y & ~TM)));
      const f32x4 od = dec4(ov);
      const float o0 = sum8(f * od.x), o1 = sum8(f * od.y), o2 = sum8(f * od.z), o3 = sum8(f * od.w);
      if ((lane & 7) == 0) *(LAS u32x4*)(L + L_CO + (lane >> 3) * 4) = u32x4{pk(o0, o0), pk(o1, o1), pk(o2, o2), pk(o3, o3)};
      lds_signal(L + F_O);
      TS(7);
    } else if (ww == 1) {
      if (!isq && !(ABL & (2 | 32))) {
        lds_wait<4>(L + F_V, 2);
        u32x4 p[3];
        int off[3];
#pragma unroll
        for (int i = 0; i < 3; ++i) {
          const int it = i * 8 + (lane >> 3), m2 = it / 12, rem = it - 12 * m2;
          off[i] = WS_GU + gi * 3072 + ((m2 * 8 + (lane & 7)) * 12 + rem) * 16;
        }
#pragma unroll 1
        for (unsigned spin = 0; spin < SPIN_CAP; ++spin) {
          CBAR();
#pragma unroll
          for (int i = 0; i < 3; ++i) p[i] = ld_sc(rw, off[i], 0);
          if (__all(((diff4(p[0], t10) | diff4(p[1], t10) | diff4(p[2], t10)) & TM) == 0)) break;
          __builtin_amdgcn_s_sleep(QSLEEP);
        }
        TS(4);
#pragma unroll
        for (int i = 0; i < 3; ++i) {
          const f32x4 d = dec4(p[i]);
          const f32x4 t = {sum8(d.x), sum8(d.y), sum8(d.z), sum8(d.w)};
          if ((lane & 7) == 0) *(LAS f32x4*)(L + L_GU + (i * 8 + (lane >> 3)) * 4) = t;
        }
        __builtin_amdgcn_fence(__ATOMIC_ACQ_REL, "wavefront", "local");
        if (lane < 48) {
          const float h = rbf(asf(L[L_GU + lane])) * rbf(asf(L[L_GU + 48 + lane]));
          const int b = lane >= 24, qr = lane - 24 * b;
          L[L_CH + b * 24 + (qr & 3) * 6 + (qr >> 2)] = pk(h, h);
        }
      }
      lds_signal(L + F_H);
      TS(5);
      lds_wait<4>(L + F_V, 3);
#pragma unroll
      for (int i = 0; i < 4; ++i) W3[i] = ld_nt(ro, wo_off, i * (D * 2));
#pragma unroll
      for (int i = 0; i < 6; ++i) W3[4 + i] = ld_nt(rd, wd_off, wd_row(i));
      __builtin_amdgcn_sched_barrier(0);
    }
  }

  if (ABL & (4 | 64 | 128)) {
    if (w < 4 || w == 5)
#pragma unroll
    for (int i = 0; i < 10; ++i) { const u32x4 t = W3[i]; asm volatile("" :: "v"(t)); }
    TSFLUSH(); return; }
  if (w < 4 || w == 5) {
    if (!isq) {
      lds_wait<1>(L + F_H, 1);
      u32x2 ch[3];
#pragma unroll
      for (int i = 0; i < 3; ++i) ch[i] = *(LAS u32x2*)(L + L_CH + sel * 24 + G * 6 + i * 2);
#pragma unroll
      for (int i = 0; i < 6; ++i) { const unsigned t = ch[i >> 1][i & 1]; y = gemv4(t, mask, W3[4 + i], y); }
    }
    TS(6);
    lds_wait<FOS>(L + F_O, 1);
    const u32x4 co = *(LAS u32x4*)(L + L_CO + (lane >> 3) * 4);
    TS(7);
#pragma unroll
    for (int i = 0; i < 4; ++i) { const unsigned t = co[i]; y = gemv4(t, mask, W3[i], y); }
    st_sc(rw, yoff, 0, enc4(y, L[TAG_LO] & TM));
    TS(8);
  } else if (c < 72) lds_wait<2>(L + F_O, 1);

  if (c < 72) {
    const unsigned tg = L[TAG_LO] & TM;
    __builtin_amdgcn_s_sleep(FIN_DELAY);
    u32x4 p0, p1;
    const int off = WS_Y + c * 16384 + w * 2048 + lane * 16;
#pragma unroll 1
    for (unsigned spin = 0; spin < SPIN_CAP; ++spin) {
      CBAR();
      p0 = ld_sc(rw, off, 0); p1 = ld_sc(rw, off, 1024);
      if (__all(((diff4(p0, tg) | diff4(p1, tg)) & TM) == 0)) break;
    }
    TS(9);
    f32x4 t = dec4(p0) + dec4(p1);
#pragma unroll
    for (int i = 0; i < 4; ++i) { float u = t[i]; u += dpp<ROR4>(u); u += dpp<ROR8>(u); t[i] = sum_x32(sum_x16(u)); }
    if (lane < 4) *(LAS f32x4*)(L + L_FIN + w * 16 + lane * 4) = t;
    __builtin_amdgcn_fence(__ATOMIC_RELEASE, "workgroup", "local");
    unsigned old = 0;
    if (lane == 0) old = __hip_atomic_fetch_add(L + C_FIN, 1u, __ATOMIC_RELAXED, __HIP_MEMORY_SCOPE_WORKGROUP);
    if (__builtin_amdgcn_readfirstlane(old) == 7) {
      __builtin_amdgcn_fence(__ATOMIC_ACQUIRE, "workgroup", "local");
      if (lane < 4) {
        f32x4 r = *(LAS f32x4*)(L + L_FIN + lane * 4);
#pragma unroll
        for (int i = 1; i < 8; ++i) r += *(LAS f32x4*)(L + L_FIN + i * 16 + lane * 4);
        const u32x2 xr = *(LAS u32x2*)(L + L_XR + lane * 2);
        const int sg = c >> 4, g2 = sg < 4 ? (c >> 2) & 3 : (c >> 1) & 3, mm = sg < 4 ? c & 3 : c & 1;
        *(u32x2*)(a.out + (g2 >> 1) * D + 128 * sg + 8 * (4 * mm + lane) + 4 * (g2 & 1)) =
            u32x2{pk(lo(xr.x) + r.x, hi(xr.x) + r.y), pk(lo(xr.y) + r.z, hi(xr.y) + r.w)};
        if (c == 0 && lane == 0) __builtin_amdgcn_raw_buffer_store_b64(*(LAS u32x2*)(L + TAG_LO), rw, WS_E, 0, AUX_SC1);
      }
    }
    TS(10);
  }
  TSFLUSH();
  if (blockIdx.x == 0 && w == 0) { while (__builtin_amdgcn_s_memrealtime() - pad_t0 < 3000) __builtin_amdgcn_s_sleep(1); }
}

}

extern "C" void kernel_launch(void* const* d_in, const int* in_sizes, int n_in, void* d_out, int out_size, void* d_ws, size_t ws_size,
                              hipStream_t stream) {
  Args a;
  a.x = (const u16*)d_in[0]; a.k = (const u16*)d_in[1]; a.v = (const u16*)d_in[2]; a.wq = (const u16*)d_in[3]; a.wo = (const u16*)d_in[4];
  a.wg = (const u16*)d_in[6]; a.wu = (const u16*)d_in[7]; a.wd = (const u16*)d_in[8];
  a.out = (u16*)d_out; a.ws = (unsigned char*)d_ws;
  fused_decode<<<NWG, NTHR, 0, stream>>>(a);
}
```

```cpp
#include <hip/hip_runtime.h>
#include <hip/hip_bf16.h>
#include <stdint.h>

namespace {

typedef unsigned short u16;
typedef unsigned long long u64;
typedef unsigned u32x2 __attribute__((ext_vector_type(2)));
typedef unsigned u32x3 __attribute__((ext_vector_type(3)));
typedef unsigned u32x4 __attribute__((ext_vector_type(4)));
typedef float f32x2 __attribute__((ext_vector_type(2)));
typedef float f32x4 __attribute__((ext_vector_type(4)));
typedef __bf16 bf16x2 __attribute__((ext_vector_type(2)));
typedef __bf16 bf16x8 __attribute__((ext_vector_type(8)));
typedef __amdgpu_buffer_rsrc_t rsrc_t;
#define LAS __attribute__((address_space(3)))

constexpr int D = 576, SK = 4096, DH = 128, F = 4608, NQ = 4096;
constexpr int NWG = 256, NTHR = 512;
constexpr int AUX_NT = 2, AUX_SC1 = 16;
constexpr int OOB = 0x7ffffff0;
constexpr unsigned TM = 0x3FFu;
constexpr unsigned SPIN_CAP = 1u << 16;
#define ABL 0
#define WIN 6
#define QK0 40
#define QKR 2
#define FV_STEP 60
#define PQ_STEP 26
#define PG_STEP 40
#define CDS 0
#define OSLEEP 0
#define FOS 0
#define PRIO2 2
#define MAP 1
#define PE 0
#define PA 200
#define PT 224
#define CPF 0
#define CODE_BYTES 16384
#define QSLEEP 16
#define FIN_DELAY 4

constexpr int WS_E = 0;
constexpr int WS_ST = 4096;
constexpr int WS_Q = 16384;
constexpr int WS_O = WS_Q + 131072;
constexpr int WS_GU = WS_O + 262144;
constexpr int WS_Y = WS_GU + 192 * 3072;
constexpr int WS_END = WS_Y + 72 * 16384;

constexpr int L_CNT = 0;
enum { C_QP = 0, F_Q, C_ST, C_D, F_H, F_O, F_V, C_FIN, TAG_LO, TAG_HI, N_CNT = 8 };
constexpr int L_Q = 16;
constexpr int L_ST = L_Q + 128;
constexpr int L_CH = L_ST + 16;
constexpr int L_CO = L_CH + 48;
constexpr int L_XR = L_CO + 32;
constexpr int L_GU = L_XR + 8;
constexpr int L_FIN = L_GU + 96;
constexpr int L_D = L_FIN + 128;
constexpr int L_QP = L_D + 1024;
constexpr int L_END = L_QP + 1024;

struct Args { const u16 *x, *k, *v, *wq, *wo, *wg, *wu, *wd; u16* out; unsigned char* ws; };

template <int I> struct IC { static constexpr int value = I; };
template <int N, int I = 0, class Fn> __device__ __forceinline__ void static_for(Fn&& f) {
  if constexpr (I < N) { f(IC<I>{}); static_for<N, I + 1>(f); }
}
__device__ __forceinline__ rsrc_t mk(const void* p, int bytes) { return __builtin_amdgcn_make_buffer_rsrc((void*)p, 0, bytes, 0x00020000); }
__device__ __forceinline__ u32x4 ld_nt(rsrc_t r, int voff, int soff) { return __builtin_amdgcn_raw_buffer_load_b128(r, voff, soff, AUX_NT); }
__device__ __forceinline__ u32x4 ld_sc(rsrc_t r, int voff, int soff) { return __builtin_amdgcn_raw_buffer_load_b128(r, voff, soff, AUX_SC1); }
__device__ __forceinline__ void st_sc(rsrc_t r, int voff, int soff, u32x4 v) { __builtin_amdgcn_raw_buffer_store_b128(v, r, voff, soff, AUX_SC1); }
__device__ __forceinline__ float asf(unsigned u) { return __builtin_bit_cast(float, u); }
__device__ __forceinline__ unsigned asu(float f) { return __builtin_bit_cast(unsigned, f); }
__device__ __forceinline__ float lo(unsigned v) { return asf(v << 16); }
__device__ __forceinline__ float hi(unsigned v) { return asf(v & 0xffff0000u); }
__device__ __forceinline__ unsigned pk(float a, float b) { const f32x2 v = {a, b}; return __builtin_bit_cast(unsigned, __builtin_convertvector(v, bf16x2)); }
__device__ __forceinline__ float rbf(float a) { return lo(pk(a, a)); }
__device__ __forceinline__ float dot2(unsigned a, unsigned b, float c) {
  return __builtin_amdgcn_fdot2_f32_bf16(__builtin_bit_cast(bf16x2, a), __builtin_bit_cast(bf16x2, b), c, false);
}
template <int CTRL> __device__ __forceinline__ float dpp(float x) {
  return asf(__builtin_amdgcn_update_dpp(0, (int)asu(x), CTRL, 0xf, 0xf, true));
}
constexpr int XOR1 = 0xB1, XOR2 = 0x4E, HMIR = 0x141, MIR = 0x140, ROR4 = 0x124, ROR8 = 0x128;
__device__ __forceinline__ float sum8(float a) { a += dpp<XOR1>(a); a += dpp<XOR2>(a); return a + dpp<HMIR>(a); }
__device__ __forceinline__ float max8(float a) { a = fmaxf(a, dpp<XOR1>(a)); a = fmaxf(a, dpp<XOR2>(a)); return fmaxf(a, dpp<HMIR>(a)); }
__device__ __forceinline__ float sum_x16(float a) { auto s = __builtin_amdgcn_permlane16_swap(asu(a), asu(a), false, false); return asf(s[0]) + asf(s[1]); }
__device__ __forceinline__ float sum_x32(float a) { auto s = __builtin_amdgcn_permlane32_swap(asu(a), asu(a), false, false); return asf(s[0]) + asf(s[1]); }
__device__ __forceinline__ float max_x16(float a) { auto s = __builtin_amdgcn_permlane16_swap(asu(a), asu(a), false, false); return fmaxf(asf(s[0]), asf(s[1])); }
__device__ __forceinline__ float max_x32(float a) { auto s = __builtin_amdgcn_permlane32_swap(asu(a), asu(a), false, false); return fmaxf(asf(s[0]), asf(s[1])); }
__device__ __forceinline__ f32x4 gemv4(unsigned coef2, const u32x4& mask, const u32x4& w, f32x4 acc) {
  const u32x4 a = {coef2 & mask.x, coef2 & mask.y, coef2 & mask.z, coef2 & mask.w};
  return __builtin_amdgcn_mfma_f32_16x16x32_bf16(__builtin_bit_cast(bf16x8, a), __builtin_bit_cast(bf16x8, w), acc, 0, 0, 0);
}
__device__ __forceinline__ unsigned enc(float v, unsigned t10) { return ((asu(v) + 0x200u) & ~TM) | t10; }
__device__ __forceinline__ u32x4 enc4(f32x4 v, unsigned t10) { return u32x4{enc(v.x, t10), enc(v.y, t10), enc(v.z, t10), enc(v.w, t10)}; }
__device__ __forceinline__ f32x4 dec4(const u32x4& v) { return f32x4{asf(v.x & ~TM), asf(v.y & ~TM), asf(v.z & ~TM), asf(v.w & ~TM)}; }
__device__ __forceinline__ unsigned diff4(const u32x4& v, unsigned t) { return ((v.x ^ t) | (v.y ^ t)) | ((v.z ^ t) | (v.w ^ t)); }
#define CBAR() asm volatile("" ::: "memory")
#define DRAIN_VM() asm volatile("s_waitcnt vmcnt(0)" ::: "memory")
template <int N> __device__ __forceinline__ void throttle() { asm volatile("s_waitcnt vmcnt(%0)" ::"i"(N) : "memory"); }
__device__ __forceinline__ void lds_signal(LAS unsigned* p) {
  __builtin_amdgcn_fence(__ATOMIC_RELEASE, "workgroup", "local");
  if ((threadIdx.x & 63) == 0) __hip_atomic_fetch_add(p, 1u, __ATOMIC_RELAXED, __HIP_MEMORY_SCOPE_WORKGROUP);
  CBAR();
}
template <int SLEEP> __device__ __forceinline__ void lds_wait(LAS unsigned* p, unsigned n) {
  while ((unsigned)__builtin_amdgcn_readfirstlane(__hip_atomic_load(p, __ATOMIC_RELAXED, __HIP_MEMORY_SCOPE_WORKGROUP)) < n)
    if (SLEEP) __builtin_amdgcn_s_sleep(SLEEP);
  __builtin_amdgcn_fence(__ATOMIC_ACQUIRE, "workgroup", "local");
}
#define TS(i)
#define TSDECL
#define TSFLUSH()

__device__ __forceinline__ void normalize18(const u32x3 (&r)[3], float m, float sd, float rs, unsigned (&o)[18]) {
#pragma unroll
  for (int i = 0; i < 9; ++i) {
    const unsigned t = r[i / 3][i % 3];
    const unsigned d = pk(lo(t) - m, hi(t) - m);
    const float d0 = lo(d), d1 = hi(d);
    float q0 = d0 * rs, q1 = d1 * rs;
    q0 = fmaf(fmaf(-q0, sd, d0), rs, q0);
    q1 = fmaf(fmaf(-q1, sd, d1), rs, q1);
    o[2 * i] = pk(q0, q0);
    o[2 * i + 1] = pk(q1, q1);
  }
}

__global__ void __launch_bounds__(NTHR, 2) fused_decode(Args a) {
  __shared__ __attribute__((aligned(16))) unsigned lds_raw[L_END];
  LAS unsigned* L = (LAS unsigned*)lds_raw;
  TSDECL;
  TS(0);
  const u64 pc0 = __builtin_amdgcn_s_getpc() & ~(u64)255;
  const int lane = threadIdx.x & 63, w = __builtin_amdgcn_readfirstlane(threadIdx.x >> 6);
  const int c = blockIdx.x, s = MAP ? c & 7 : (c >> 3) & 7, pair = MAP ? c >> 3 : (c & 7) * 4 + (c >> 6), bb = pair & 1, kvh = pair >> 1;
  const int sr = MAP ? (s + pair) & 7 : s;
  const bool isq = (sr & 3) == 0;
  const int qi = pair * 2 + (sr >> 2), gi = pair * 6 + (sr >> 2) * 3 + (sr & 3) - 1;
  const int G = lane >> 4, cc = lane & 15, sel = (lane >> 3) & 1;
  const rsrc_t rw = mk(a.ws, WS_END);
  const u32x4 mask = {(lane & 7) == 0 ? 0xffffu : (lane & 7) == 1 ? 0xffff0000u : 0u, (lane & 7) == 2 ? 0xffffu : (lane & 7) == 3 ? 0xffff0000u : 0u,
                      (lane & 7) == 4 ? 0xffffu : (lane & 7) == 5 ? 0xffff0000u : 0u, (lane & 7) == 6 ? 0xffffu : (lane & 7) == 7 ? 0xffff0000u : 0u};
  const int j = bb * 8 + s;
  const int seg = w < 4 ? w : 4;
  const bool segok = seg < 4 || cc < 8;
  const int yoff = segok ? WS_Y + ((seg * 16 + (seg < 4 ? (lane >> 2) : G * 2 + (cc >> 2))) * 256 + c) * 64 + (lane & 3) * 16 : OOB;
  const int wo_off = segok ? (kvh * 256 + (j >> 3) * 128 + 8 * (4 * (j & 3) + G) + 4 * ((j >> 2) & 1)) * (D * 2) + seg * 256 + cc * 16 : OOB;
  const int wd_off = segok && !isq ? G * (D * 2) + seg * 256 + cc * 16 : OOB;
  auto wd_row = [&](int i) { const int li = gi * 6 + i; return (128 * (li >> 5) + 8 * (li & 15) + 4 * ((li >> 4) & 1)) * (D * 2); };
  const rsrc_t ro = mk(a.wo, NQ * D * 2), rd = mk(a.wd, F * D * 2);
  u32x4 W3[10];
  f32x4 y = {0.f, 0.f, 0.f, 0.f};

  if (w < 4) {
    const unsigned tstart = PE ? (unsigned)__builtin_amdgcn_s_memrealtime() : 0u;
    if (w == 0 && lane < N_CNT) L[L_CNT + lane] = 0u;
    unsigned cpf = 0;
    if (CPF == 1 && w == 1) cpf = __builtin_amdgcn_raw_buffer_load_b32(mk((const void*)pc0, CODE_BYTES), lane < 5 ? ((c >> 3) * 5 + lane) * 128 : OOB, 0, 0);
    if (CPF == 2 && w >= 1) cpf = __builtin_amdgcn_raw_buffer_load_b32(mk((const void*)pc0, CODE_BYTES), ((w - 1) * 64 + lane) * 128, 0, 0);
    const size_t kvbase = ((size_t)(bb * 16 + kvh) * SK + s * 512) * DH;
    const rsrc_t rk = mk(a.k + kvbase, 512 * DH * 2), rv = mk(a.v + kvbase, 512 * DH * 2);
    const int voff = w * 1024 + lane * 16;
    asm volatile("s_waitcnt lgkmcnt(0)" ::: "memory");
    __builtin_amdgcn_s_barrier();
    CBAR();
    TS(1);
    u32x4 K[32], V[32], qa, qb;
    float sc[8];
    unsigned pp[8];
    f32x4 acc = {0.f, 0.f, 0.f, 0.f};
    float mx = -INFINITY, lsum = 0.f;
    constexpr int PV0 = QK0 + 32 / QKR;
    static_for<74>([&](auto ic) {
      constexpr int n = ic.value;
      if constexpr (n >= WIN) throttle<WIN - 1>();
      if constexpr (PE > 0 && n % (PE > 0 ? PE : 1) == 0) { while ((int)((unsigned)__builtin_amdgcn_s_memrealtime() - tstart) < PA + ((n * PT) >> 4)) {} }
      if constexpr (n < 32) K[n] = ld_nt(rk, voff, n * 4096);
      else if constexpr (n < 64) V[n - 32] = ld_nt(rv, voff, (n - 32) * 4096);
      else if constexpr (n < 68) W3[n - 64] = ld_nt(ro, wo_off, (n - 64) * (D * 2));
      else W3[n - 64] = ld_nt(rd, wd_off, wd_row(n - 68));
      if constexpr (n == PQ_STEP || n == PG_STEP || n == FV_STEP) { if (w == 0) lds_signal(L + F_V); }
      if constexpr (n == QK0) {
        if (CPF) asm volatile("" :: "v"(cpf));
        TS(2);
        if (!(ABL & 4)) lds_wait<1>(L + F_Q, 1);
        qa = *(LAS u32x4*)(L + L_Q + sel * 64 + cc * 4);
        qb = *(LAS u32x4*)(L + L_Q + (sel ^ 1) * 64 + cc * 4);
        TS(3);
      }
      if constexpr (n >= QK0 && n < PV0 && (n - QK0) % (4 / QKR) == 0) {
        constexpr int g = (n - QK0) * QKR / 4;
        float t[4];
#pragma unroll
        for (int e = 0; e < 4; ++e) {
          float u = 0.f, v = 0.f;
#pragma unroll
          for (int d = 0; d < 4; ++d) { const unsigned kk = K[4 * g + e][d], q0 = qa[d], q1 = qb[d]; u = dot2(kk, q0, u); v = dot2(kk, q1, v); }
          t[e] = u + dpp<ROR8>(v);
          asm volatile("" : "+v"(t[e]));
          t[e] += dpp<HMIR>(t[e]);
          asm volatile("" : "+v"(t[e]));
        }
        const bool o1 = lane & 1, o2 = lane & 2;
        float r0 = (o1 ? t[1] : t[0]) + dpp<XOR1>(o1 ? t[0] : t[1]);
        asm volatile("" : "+v"(r0));
        float r1 = (o1 ? t[3] : t[2]) + dpp<XOR1>(o1 ? t[2] : t[3]);
        const float r = (o2 ? r1 : r0) + dpp<XOR2>(o2 ? r0 : r1);
        sc[g] = rbf(rbf(r) * 0.08838834764831845f);
        mx = fmaxf(mx, sc[g]);
      }
      if constexpr (n == PV0 - 1) {
        mx = fmaxf(mx, dpp<XOR1>(mx)); mx = fmaxf(mx, dpp<XOR2>(mx));
        mx = max_x32(max_x16(mx));
        const float mneg = -mx * 1.4426950408889634f;
#pragma unroll
        for (int i = 0; i < 8; ++i) { const float e = __builtin_amdgcn_exp2f(fmaf(sc[i], 1.4426950408889634f, mneg)); lsum += e; pp[i] = pk(e, e); }
        lsum += dpp<XOR1>(lsum); lsum += dpp<XOR2>(lsum);
        lsum = sum_x32(sum_x16(lsum));
        if ((lane & 0x37) == 0) *(LAS f32x2*)(L + L_ST + w * 4 + sel * 2) = f32x2{mx, lsum};
        lds_signal(L + C_ST);
        TS(4);
      }
      if constexpr (n >= PV0) {
        constexpr int done = (n - PV0) * 4 < n - 32 - WIN ? (n - PV0) * 4 : n - 32 - WIN;
        constexpr int upto = (n - PV0 + 1) * 4 < n - 32 - WIN + 1 ? (n - PV0 + 1) * 4 : n - 32 - WIN + 1;
        static_for<32>([&](auto jc) {
          constexpr int i = jc.value;
          if constexpr (i >= done && i < upto) acc = gemv4(__builtin_amdgcn_update_dpp(0u, pp[i >> 2], (i & 3) * 0x55, 0xf, 0xf, true), mask, V[i], acc);
          if constexpr (i == 31 && i >= done && i < upto) {
            TS(5);
            *(LAS f32x4*)(L + L_D + w * 256 + lane * 4) = acc;
            lds_signal(L + C_D);
          }
        });
      }
      __builtin_amdgcn_sched_barrier(0);
    });
    if constexpr (73 - 32 - WIN + 1 < 32) {
      static_for<32>([&](auto jc) {
        constexpr int i = jc.value;
        if constexpr (i >= 73 - 32 - WIN + 1) acc = gemv4(__builtin_amdgcn_update_dpp(0u, pp[i >> 2], (i & 3) * 0x55, 0xf, 0xf, true), mask, V[i], acc);
      });
      TS(5);
      *(LAS f32x4*)(L + L_D + w * 256 + lane * 4) = acc;
      lds_signal(L + C_D);
    }
  } else {
    __builtin_amdgcn_s_setprio(2);
    const int ww = w - 4;
    const rsrc_t rx = mk(a.x, 2 * D * 2);
    const u32x2 ev = __builtin_amdgcn_raw_buffer_load_b64(rw, WS_E, 0, AUX_SC1);
    u32x3 xs[3], x0[3];
#pragma unroll
    for (int i = 0; i < 3; ++i) xs[i] = __builtin_amdgcn_raw_buffer_load_b96(rx, lane * 36 + i * 12, 0, 0);
    const int sl = isq ? qi * 4 + ww : gi * 3 + ww;
    const bool act = isq || ww < 3;
    const int r0 = 72 * (sl & 7) + 18 * G;
    const int blk = sl >> 3, mat = blk >= 36, cb = blk - 36 * mat;
#pragma unroll
    for (int i = 0; i < 3; ++i) x0[i] = __builtin_amdgcn_raw_buffer_load_b96(rx, sel * (D * 2) + r0 * 2 + i * 12, 0, 0);
    const int rstride = isq ? NQ * 2 : F * 2;
    const rsrc_t r1 = mk(isq ? a.wq : mat ? a.wu : a.wg, D * rstride);
    const int woff = act ? r0 * rstride + cb * 256 + cc * 16 : OOB;
    u32x4 Wt[18];
#pragma unroll
    for (int i = 0; i < 18; ++i) { Wt[i] = ld_nt(r1, woff, i * rstride); __builtin_amdgcn_sched_barrier(0); }
    __builtin_amdgcn_s_barrier();
    CBAR();
    TS(1);
    float sm = 0.f;
#pragma unroll
    for (int i = 0; i < 9; ++i) { const unsigned t = xs[i / 3][i % 3]; sm += lo(t) + hi(t); }
    sm = sum8(sm); sm += dpp<MIR>(sm); sm = sum_x16(sm);
    const float mean = sm * (1.f / D);
    float sv = 0.f;
#pragma unroll
    for (int i = 0; i < 9; ++i) { const unsigned t = xs[i / 3][i % 3]; const float d0 = lo(t) - mean, d1 = hi(t) - mean; sv = fmaf(d0, d0, fmaf(d1, d1, sv)); }
    sv = sum8(sv); sv += dpp<MIR>(sv); sv = sum_x16(sv);
    const float mb = rbf(mean), sd = rbf(sqrtf(rbf(rbf(sv * (1.f / D)) + 1e-5f))), rs = __builtin_amdgcn_rcpf(sd);
    const float m0 = asf(__builtin_amdgcn_readlane(asu(mb), 0)), m1 = asf(__builtin_amdgcn_readlane(asu(mb), 32));
    const float s0 = asf(__builtin_amdgcn_readlane(asu(sd), 0)), s1 = asf(__builtin_amdgcn_readlane(asu(sd), 32));
    const float i0 = asf(__builtin_amdgcn_readlane(asu(rs), 0)), i1 = asf(__builtin_amdgcn_readlane(asu(rs), 32));
    const u64 tag = (((u64)(unsigned)__builtin_amdgcn_readfirstlane(ev.y) << 32) | (unsigned)__builtin_amdgcn_readfirstlane(ev.x)) + 1;
    const unsigned tlo = (unsigned)tag, thi = (unsigned)(tag >> 32), t16 = tlo & 0xffffu, t10 = tlo & TM;
    if (act) {
      unsigned c0[18];
      normalize18(x0, sel ? m1 : m0, sel ? s1 : s0, sel ? i1 : i0, c0);
      f32x4 d0 = {0.f, 0.f, 0.f, 0.f};
#pragma unroll
      for (int i = 0; i < 18; ++i) d0 = gemv4(c0[i], mask, Wt[i], d0);
      TS(2);
      if (isq) {
        *(LAS f32x4*)(L + L_QP + ww * 256 + lane * 4) = d0;
        lds_signal(L + C_QP);
        if (ww == 0) {
          lds_wait<0>(L + C_QP, 4);
#pragma unroll
          for (int i = 1; i < 4; ++i) d0 += *(LAS f32x4*)(L + L_QP + i * 256 + lane * 4);
          const int qo = WS_Q + ((((qi >> 2) * 2 + (lane >> 5)) * 4 + (qi & 3)) * 64 + (lane & 31)) * 16;
          st_sc(rw, qo, 0, u32x4{asu(d0.x), asu(d0.y), tlo, thi});
          st_sc(rw, qo, 512, u32x4{asu(d0.z), asu(d0.w), tlo, thi});
        }
      } else if (!(ABL & 2)) {
        const int li = cb * 32 + (lane & 31), cons = li / 6;
        st_sc(rw, WS_GU + cons * 3072 + (((mat * 8 + (sl & 7)) * 2 + (lane >> 5)) * 6 + (li - cons * 6)) * 16, 0, enc4(d0, t10));
      } else asm volatile("" :: "v"(d0));
    }
    if (ww == 3 && c < 72 && lane < 4) {
      const int sg = c >> 4, g2 = sg < 4 ? (c >> 2) & 3 : (c >> 1) & 3, mm = sg < 4 ? c & 3 : c & 1;
      *(LAS u32x2*)(L + L_XR + lane * 2) = __builtin_amdgcn_raw_buffer_load_b64(rx, ((g2 >> 1) * D + 128 * sg + 8 * (4 * mm + lane) + 4 * (g2 & 1)) * 2, 0, 0);
    }
    if (PRIO2 != 2) __builtin_amdgcn_s_setprio(PRIO2);
    TS(3);
    if (ABL & 4) { TSFLUSH(); return; }
    if (ww == 0) {
      lds_wait<4>(L + F_V, 1);
      u32x4 g[4];
      const int qoff = WS_Q + (kvh * 2 + bb) * 4096 + lane * 16;
#pragma unroll 1
      for (unsigned spin = 0; spin < SPIN_CAP; ++spin) {
        CBAR();
        unsigned bad = 0;
        if (ABL & 16) { for (int i = 0; i < 4; ++i) g[i] = u32x4{tlo, tlo, tlo, tlo}; break; }
#pragma unroll
        for (int i = 0; i < 4; ++i) g[i] = ld_sc(rw, qoff, i * 1024);
#pragma unroll
        for (int i = 0; i < 4; ++i) bad |= (g[i].z ^ tlo) | (g[i].w ^ thi);
        if (__all(bad == 0)) break;
        __builtin_amdgcn_s_sleep(QSLEEP);
      }
#pragma unroll
      for (int h = 0; h < 2; ++h)
        L[L_Q + h * 64 + cc * 4 + (G & 1) * 2 + (lane >> 5)] = pk(asf(g[2 * h].x) + asf(g[2 * h + 1].x), asf(g[2 * h].y) + asf(g[2 * h + 1].y));
      if (lane == 0) *(LAS u32x2*)(L + TAG_LO) = u32x2{tlo, thi};
      lds_signal(L + F_Q);
      TS(4);
      if (ABL & 64) { TSFLUSH(); return; }
      const int ooff = WS_O + ((kvh * 16 + (lane >> 2)) * 16 + j) * 64 + (lane & 3) * 16;
      const int goff = WS_O + ((kvh * 16 + j) * 16 + cc) * 64 + G * 16, moff = WS_ST + ((kvh * 2 + (j >> 3)) * 16 + cc) * 16;
      lds_wait<4>(L + C_ST, 4);
      f32x2 st[4];
#pragma unroll
      for (int i = 0; i < 4; ++i) st[i] = *(LAS f32x2*)(L + L_ST + i * 4 + (lane >> 5) * 2);
      const float M = fmaxf(fmaxf(st[0].x, st[1].x), fmaxf(st[2].x, st[3].x));
      float fw[4], Ls = 0.f;
#pragma unroll
      for (int i = 0; i < 4; ++i) { fw[i] = __builtin_amdgcn_exp2f((st[i].x - M) * 1.4426950408889634f); Ls = fmaf(fw[i], st[i].y, Ls); }
      st_sc(rw, (lane & 31) == 0 ? WS_ST + ((kvh * 2 + (lane >> 5)) * 16 + j) * 16 : OOB, 0,
            u32x4{(asu(M) & 0xffff0000u) | t16, enc(Ls, t10), tlo, thi});
      TS(5);
      lds_wait<4>(L + F_V, 3);
      lds_wait<CDS>(L + C_D, 4);
      f32x4 o = {0.f, 0.f, 0.f, 0.f};
#pragma unroll
      for (int i = 0; i < 4; ++i) o += fw[i] * *(LAS f32x4*)(L + L_D + i * 256 + lane * 4);
      st_sc(rw, ooff, 0, enc4(o, t10));
      TS(6);
      if (ABL & 128) { TSFLUSH(); return; }
      u32x4 ov, ml;
#pragma unroll 1
      for (unsigned spin = 0; spin < SPIN_CAP; ++spin) {
        CBAR();
        ml = ld_sc(rw, moff, 0);
        ov = ld_sc(rw, goff, 0);
        if (__all((((diff4(ov, t10) | (ml.y ^ t10)) & TM) | ((ml.x ^ t16) & 0xffffu) | (ml.z ^ tlo) | (ml.w ^ thi)) == 0)) break;
        if (OSLEEP) __builtin_amdgcn_s_sleep(OSLEEP);
      }
      const float ms = hi(ml.x), e = __builtin_amdgcn_exp2f((ms - max8(ms)) * 1.4426950408889634f);
      const float f = e * __builtin_amdgcn_rcpf(sum8(e * asf(ml.y & ~TM)));
      const f32x4 od = dec4(ov);
      const float o0 = sum8(f * od.x), o1 = sum8(f * od.y), o2 = sum8(f * od.z), o3 = sum8(f * od.w);
      if ((lane & 7) == 0) *(LAS u32x4*)(L + L_CO + (lane >> 3) * 4) = u32x4{pk(o0, o0), pk(o1, o1), pk(o2, o2), pk(o3, o3)};
      lds_signal(L + F_O);
      TS(7);
    } else if (ww == 1) {
      if (!isq && !(ABL & (2 | 32))) {
        lds_wait<4>(L + F_V, 2);
        u32x4 p[3];
        int off[3];
#pragma unroll
        for (int i = 0; i < 3; ++i) {
          const int it = i * 8 + (lane >> 3), m2 = it / 12, rem = it - 12 * m2;
          off[i] = WS_GU + gi * 3072 + ((m2 * 8 + (lane & 7)) * 12 + rem) * 16;
        }
#pragma unroll 1
        for (unsigned spin = 0; spin < SPIN_CAP; ++spin) {
          CBAR();
#pragma unroll
          for (int i = 0; i < 3; ++i) p[i] = ld_sc(rw, off[i], 0);
          if (__all(((diff4(p[0], t10) | diff4(p[1], t10) | diff4(p[2], t10)) & TM) == 0)) break;
          __builtin_amdgcn_s_sleep(QSLEEP);
        }
        TS(4);
#pragma unroll
        for (int i = 0; i < 3; ++i) {
          const f32x4 d = dec4(p[i]);
          const f32x4 t = {sum8(d.x), sum8(d.y), sum8(d.z), sum8(d.w)};
          if ((lane & 7) == 0) *(LAS f32x4*)(L + L_GU + (i * 8 + (lane >> 3)) * 4) = t;
        }
        __builtin_amdgcn_fence(__ATOMIC_ACQ_REL, "wavefront", "local");
        if (lane < 48) {
          const float h = rbf(asf(L[L_GU + lane])) * rbf(asf(L[L_GU + 48 + lane]));
          const int b = lane >= 24, qr = lane - 24 * b;
          L[L_CH + b * 24 + (qr & 3) * 6 + (qr >> 2)] = pk(h, h);
        }
      }
      lds_signal(L + F_H);
      TS(5);
      lds_wait<4>(L + F_V, 3);
#pragma unroll
      for (int i = 0; i < 4; ++i) W3[i] = ld_nt(ro, wo_off, i * (D * 2));
#pragma unroll
      for (int i = 0; i < 6; ++i) W3[4 + i] = ld_nt(rd, wd_off, wd_row(i));
      __builtin_amdgcn_sched_barrier(0);
    }
  }

  if (ABL & (4 | 64 | 128)) {
    if (w < 4 || w == 5)
#pragma unroll
    for (int i = 0; i < 10; ++i) { const u32x4 t = W3[i]; asm volatile("" :: "v"(t)); }
    TSFLUSH(); return; }
  if (w < 4 || w == 5) {
    if (!isq) {
      lds_wait<1>(L + F_H, 1);
      u32x2 ch[3];
#pragma unroll
      for (int i = 0; i < 3; ++i) ch[i] = *(LAS u32x2*)(L + L_CH + sel * 24 + G * 6 + i * 2);
#pragma unroll
      for (int i = 0; i < 6; ++i) { const unsigned t = ch[i >> 1][i & 1]; y = gemv4(t, mask, W3[4 + i], y); }
    }
    TS(6);
    lds_wait<FOS>(L + F_O, 1);
    const u32x4 co = *(LAS u32x4*)(L + L_CO + (lane >> 3) * 4);
    TS(7);
#pragma unroll
    for (int i = 0; i < 4; ++i) { const unsigned t = co[i]; y = gemv4(t, mask, W3[i], y); }
    st_sc(rw, yoff, 0, enc4(y, L[TAG_LO] & TM));
    TS(8);
  } else if (c < 72) lds_wait<2>(L + F_O, 1);

  if (c < 72) {
    const unsigned tg = L[TAG_LO] & TM;
    __builtin_amdgcn_s_sleep(FIN_DELAY);
    u32x4 p0, p1;
    const int off = WS_Y + c * 16384 + w * 2048 + lane * 16;
#pragma unroll 1
    for (unsigned spin = 0; spin < SPIN_CAP; ++spin) {
      CBAR();
      p0 = ld_sc(rw, off, 0); p1 = ld_sc(rw, off, 1024);
      if (__all(((diff4(p0, tg) | diff4(p1, tg)) & TM) == 0)) break;
    }
    TS(9);
    f32x4 t = dec4(p0) + dec4(p1);
#pragma unroll
    for (int i = 0; i < 4; ++i) { float u = t[i]; u += dpp<ROR4>(u); u += dpp<ROR8>(u); t[i] = sum_x32(sum_x16(u)); }
    if (lane < 4) *(LAS f32x4*)(L + L_FIN + w * 16 + lane * 4) = t;
    __builtin_amdgcn_fence(__ATOMIC_RELEASE, "workgroup", "local");
    unsigned old = 0;
    if (lane == 0) old = __hip_atomic_fetch_add(L + C_FIN, 1u, __ATOMIC_RELAXED, __HIP_MEMORY_SCOPE_WORKGROUP);
    if (__builtin_amdgcn_readfirstlane(old) == 7) {
      __builtin_amdgcn_fence(__ATOMIC_ACQUIRE, "workgroup", "local");
      if (lane < 4) {
        f32x4 r = *(LAS f32x4*)(L + L_FIN + lane * 4);
#pragma unroll
        for (int i = 1; i < 8; ++i) r += *(LAS f32x4*)(L + L_FIN + i * 16 + lane * 4);
        const u32x2 xr = *(LAS u32x2*)(L + L_XR + lane * 2);
        const int sg = c >> 4, g2 = sg < 4 ? (c >> 2) & 3 : (c >> 1) & 3, mm = sg < 4 ? c & 3 : c & 1;
        *(u32x2*)(a.out + (g2 >> 1) * D + 128 * sg + 8 * (4 * mm + lane) + 4 * (g2 & 1)) =
            u32x2{pk(lo(xr.x) + r.x, hi(xr.x) + r.y), pk(lo(xr.y) + r.z, hi(xr.y) + r.w)};
        if (c == 0 && lane == 0) __builtin_amdgcn_raw_buffer_store_b64(*(LAS u32x2*)(L + TAG_LO), rw, WS_E, 0, AUX_SC1);
      }
    }
    TS(10);
  }
  TSFLUSH();
}

}

extern "C" void kernel_launch(void* const* d_in, const int* in_sizes, int n_in, void* d_out, int out_size, void* d_ws, size_t ws_size,
                              hipStream_t stream) {
  Args a;
  a.x = (const u16*)d_in[0]; a.k = (const u16*)d_in[1]; a.v = (const u16*)d_in[2]; a.wq = (const u16*)d_in[3]; a.wo = (const u16*)d_in[4];
  a.wg = (const u16*)d_in[6]; a.wu = (const u16*)d_in[7]; a.wd = (const u16*)d_in[8];
  a.out = (u16*)d_out; a.ws = (unsigned char*)d_ws;
  fused_decode<<<NWG, NTHR, 0, stream>>>(a);
}
```

```cpp
#define K_TLB 0
#include <hip/hip_runtime.h>
#include <hip/hip_bf16.h>
#include <stdint.h>

namespace {

typedef unsigned short u16;
typedef unsigned long long u64;
typedef unsigned u32x2 __attribute__((ext_vector_type(2)));
typedef unsigned u32x3 __attribute__((ext_vector_type(3)));
typedef unsigned u32x4 __attribute__((ext_vector_type(4)));
typedef unsigned u32x16 __attribute__((ext_vector_type(16)));
typedef float f32x2 __attribute__((ext_vector_type(2)));
typedef float f32x4 __attribute__((ext_vector_type(4)));
typedef __bf16 bf16x2 __attribute__((ext_vector_type(2)));
typedef __bf16 bf16x8 __attribute__((ext_vector_type(8)));
typedef __amdgpu_buffer_rsrc_t rsrc_t;
#define LAS __attribute__((address_space(3)))

constexpr int D = 576, SK = 4096, DH = 128, F = 4608, NQ = 4096;
constexpr int NWG = 256, NTHR = 512;
constexpr int AUX_NT = 2, AUX_SC1 = 16;
constexpr int OOB = 0x7ffffff0;
constexpr unsigned TM = 0x3FFu;
constexpr unsigned SPIN_CAP = 1u << 16;
constexpr float LOG2E = 1.4426950408889634f;
#define KNOB(n, v) constexpr int n = v
#define K_WIN 5
#define K_WBIG 10
#define K_W0 4
#define K_W2 7
#define K_DL 35
#define K_LV 2
#define K_STS 1
#define K_ARG 1
#define K_FW 3
#define K_A3 0
#define K_OG 0
#define K_PA 260
#define K_PT 228
#define K_LE 2
#define K_QK0 36
#define K_PQ 22
#define K_PG 40
#define K_TAILN 2
#define K_W3WIN 9
#define K_HOLD 0
#define K_FIN 24
#define K_CODE 17920
constexpr int WIN = K_WIN;
constexpr int WBIG = K_WBIG;
constexpr int PA = K_PA, PT = K_PT, LE = K_LE;
constexpr int QK0 = K_QK0;
constexpr int PQ_STEP = K_PQ, PG_STEP = K_PG;
constexpr int TAILN = K_TAILN, W3WIN = K_W3WIN, HOLD = K_HOLD, FIN_DELAY = K_FIN, CODE_BYTES = K_CODE;

constexpr int WS_E = 0;
constexpr int WS_ST = 4096;
constexpr int WS_Q = 16384;
constexpr int WS_O = WS_Q + 65536;
constexpr int WS_GU = WS_O + 262144;
constexpr int WS_Y = WS_GU + 256 * 1280;
constexpr int WS_END = WS_Y + 72 * 16384;

enum { C_P1 = 0, F_X, F_Q, C_ST, C_D, F_OP, F_H, F_O, F_V, C_FIN, F_W, N_CNT = 11, TAG_LO = 12, TAG_HI };
constexpr int L_Q = 16;
constexpr int L_ST = L_Q + 128;
constexpr int L_CH = L_ST + 16;
constexpr int L_CO = L_CH + 64;
constexpr int L_XR = L_CO + 32;
constexpr int L_GU = L_XR + 8;
constexpr int L_FIN = L_GU + 80;
constexpr int L_D = L_FIN + 128;
constexpr int L_P1 = L_D + 1024;
constexpr int L_XN = L_P1 + 1024;
constexpr int L_END = L_XN + 1152;

struct Args { const u16 *wq, *wg, *wu, *x, *k, *v, *wo, *wd; u16* out; unsigned char* ws; };

template <int I> struct IC { static constexpr int value = I; };
template <int N, int I = 0, class Fn> __device__ __forceinline__ void static_for(Fn&& f) {
  if constexpr (I < N) { f(IC<I>{}); static_for<N, I + 1>(f); }
}
__device__ __forceinline__ rsrc_t mk(const void* p, int bytes) { return __builtin_amdgcn_make_buffer_rsrc((void*)p, 0, bytes, 0x00020000); }
__device__ __forceinline__ u32x4 ld_nt(rsrc_t r, int voff, int soff) { return __builtin_amdgcn_raw_buffer_load_b128(r, voff, soff, AUX_NT); }
__device__ __forceinline__ u32x4 ld_sc(rsrc_t r, int voff, int soff) { return __builtin_amdgcn_raw_buffer_load_b128(r, voff, soff, AUX_SC1); }
__device__ __forceinline__ void st_sc(rsrc_t r, int voff, int soff, u32x4 v) { __builtin_amdgcn_raw_buffer_store_b128(v, r, voff, soff, AUX_SC1); }
__device__ __forceinline__ float asf(unsigned u) { return __builtin_bit_cast(float, u); }
__device__ __forceinline__ unsigned asu(float f) { return __builtin_bit_cast(unsigned, f); }
__device__ __forceinline__ float lo(unsigned v) { return asf(v << 16); }
__device__ __forceinline__ float hi(unsigned v) { return asf(v & 0xffff0000u); }
__device__ __forceinline__ unsigned pk(float a, float b) { const f32x2 v = {a, b}; return __builtin_bit_cast(unsigned, __builtin_convertvector(v, bf16x2)); }
__device__ __forceinline__ float rbf(float a) { return lo(pk(a, a)); }
__device__ __forceinline__ float dot2(unsigned a, unsigned b, float c) {
  return __builtin_amdgcn_fdot2_f32_bf16(__builtin_bit_cast(bf16x2, a), __builtin_bit_cast(bf16x2, b), c, false);
}
template <int CTRL> __device__ __forceinline__ float dpp(float x) {
  return asf(__builtin_amdgcn_update_dpp(0, (int)asu(x), CTRL, 0xf, 0xf, true));
}
constexpr int XOR1 = 0xB1, XOR2 = 0x4E, HMIR = 0x141, MIR = 0x140, ROR4 = 0x124, ROR8 = 0x128;
__device__ __forceinline__ float sum8(float a) { a += dpp<XOR1>(a); a += dpp<XOR2>(a); return a + dpp<HMIR>(a); }
__device__ __forceinline__ float max8(float a) { a = fmaxf(a, dpp<XOR1>(a)); a = fmaxf(a, dpp<XOR2>(a)); return fmaxf(a, dpp<HMIR>(a)); }
__device__ __forceinline__ float sum_x16(float a) { auto s = __builtin_amdgcn_permlane16_swap(asu(a), asu(a), false, false); return asf(s[0]) + asf(s[1]); }
__device__ __forceinline__ float sum_x32(float a) { auto s = __builtin_amdgcn_permlane32_swap(asu(a), asu(a), false, false); return asf(s[0]) + asf(s[1]); }
__device__ __forceinline__ float max_x16(float a) { auto s = __builtin_amdgcn_permlane16_swap(asu(a), asu(a), false, false); return fmaxf(asf(s[0]), asf(s[1])); }
__device__ __forceinline__ float max_x32(float a) { auto s = __builtin_amdgcn_permlane32_swap(asu(a), asu(a), false, false); return fmaxf(asf(s[0]), asf(s[1])); }
__device__ __forceinline__ f32x4 gemv4(unsigned coef2, const u32x4& mask, const u32x4& w, f32x4 acc) {
  const u32x4 a = {coef2 & mask.x, coef2 & mask.y, coef2 & mask.z, coef2 & mask.w};
  return __builtin_amdgcn_mfma_f32_16x16x32_bf16(__builtin_bit_cast(bf16x8, a), __builtin_bit_cast(bf16x8, w), acc, 0, 0, 0);
}
__device__ __forceinline__ unsigned enc(float v, unsigned t10) { return ((asu(v) + 0x200u) & ~TM) | t10; }
__device__ __forceinline__ u32x4 enc4(f32x4 v, unsigned t10) { return u32x4{enc(v.x, t10), enc(v.y, t10), enc(v.z, t10), enc(v.w, t10)}; }
__device__ __forceinline__ f32x4 dec4(const u32x4& v) { return f32x4{asf(v.x & ~TM), asf(v.y & ~TM), asf(v.z & ~TM), asf(v.w & ~TM)}; }
__device__ __forceinline__ unsigned diff4(const u32x4& v, unsigned t) { return ((v.x ^ t) | (v.y ^ t)) | ((v.z ^ t) | (v.w ^ t)); }
#define CBAR() asm volatile("" ::: "memory")
#define DRAIN_VM() asm volatile("s_waitcnt vmcnt(0)" ::: "memory")
template <int N> __device__ __forceinline__ void throttle() { asm volatile("s_waitcnt vmcnt(%0)" ::"i"(N) : "memory"); }
template <int N, int NBIG, int DUE> __device__ __forceinline__ void throttle2(unsigned elapsed) {
  asm volatile("s_cmpk_gt_u32 %0, %3\n\ts_cbranch_scc1 .Lthr%=\n\ts_waitcnt vmcnt(%1)\n.Lthr%=:\n\ts_waitcnt vmcnt(%2)" ::"s"(elapsed), "i"(N), "i"(NBIG), "i"(DUE) : "scc", "memory");
}
template <int N0, int N1, int N2, int N3, int DUE, int DL> __device__ __forceinline__ void throttle4(unsigned elapsed) {
  asm volatile("s_cmpk_gt_u32 %0, %6\n\ts_cbranch_scc0 .Lta%=\n\ts_cmpk_gt_u32 %0, %7\n\ts_cbranch_scc1 .Ltd%=\n\ts_branch .Ltc%=\n"
               ".Lta%=:\n\ts_cmpk_gt_u32 %0, %5\n\ts_cbranch_scc1 .Ltb%=\n\ts_waitcnt vmcnt(%1)\n.Ltb%=:\n\ts_waitcnt vmcnt(%2)\n"
               ".Ltc%=:\n\ts_waitcnt vmcnt(%3)\n.Ltd%=:\n\ts_waitcnt vmcnt(%4)"
               ::"s"(elapsed), "i"(N0), "i"(N1), "i"(N2), "i"(N3), "i"(DUE - DL), "i"(DUE), "i"(DUE + DL) : "scc", "memory");
}
__device__ __forceinline__ void lds_signal(LAS unsigned* p) {
  __builtin_amdgcn_fence(__ATOMIC_RELEASE, "workgroup", "local");
  if ((threadIdx.x & 63) == 0) __hip_atomic_fetch_add(p, 1u, __ATOMIC_RELAXED, __HIP_MEMORY_SCOPE_WORKGROUP);
  CBAR();
}
__device__ __forceinline__ unsigned lds_arrive(LAS unsigned* p) {
  __builtin_amdgcn_fence(__ATOMIC_RELEASE, "workgroup", "local");
  unsigned old = 0;
  if ((threadIdx.x & 63) == 0) old = __hip_atomic_fetch_add(p, 1u, __ATOMIC_RELAXED, __HIP_MEMORY_SCOPE_WORKGROUP);
  old = __builtin_amdgcn_readfirstlane(old);
  __builtin_amdgcn_fence(__ATOMIC_ACQUIRE, "workgroup", "local");
  return old;
}
template <int SLEEP> __device__ __forceinline__ void lds_wait(LAS unsigned* p, unsigned n) {
  while ((unsigned)__builtin_amdgcn_readfirstlane(__hip_atomic_load(p, __ATOMIC_RELAXED, __HIP_MEMORY_SCOPE_WORKGROUP)) < n)
    if (SLEEP) __builtin_amdgcn_s_sleep(SLEEP);
  __builtin_amdgcn_fence(__ATOMIC_ACQUIRE, "workgroup", "local");
}
__device__ __forceinline__ unsigned now32() { return (unsigned)__builtin_amdgcn_s_memrealtime(); }
__device__ __forceinline__ void wait_until(unsigned t) {
  t = __builtin_amdgcn_readfirstlane(t);
  while ((unsigned)(t - now32() - 1u) < 1000u) {}
}
#define TS(i)
#define TSDECL
#define TSFLUSH()

__global__ void __launch_bounds__(NTHR, 2) fused_decode(Args a) {
  __shared__ __attribute__((aligned(16))) unsigned lds_raw[L_END];
  LAS unsigned* L = (LAS unsigned*)lds_raw;
  TSDECL;
  TS(0);
  u64 pc0, tst;
  u32x16 ka; u32x4 kb;
  asm volatile("s_load_dwordx16 %0, %4, 0x0\n\ts_load_dwordx4 %1, %4, 0x40\n\ts_getpc_b64 %2\n\ts_memrealtime %3"
               : "=&s"(ka), "=&s"(kb), "=&s"(pc0), "=&s"(tst) : "s"(__builtin_amdgcn_kernarg_segment_ptr()));
  const int lane = threadIdx.x & 63, w = __builtin_amdgcn_readfirstlane(threadIdx.x >> 6);
  const int c = blockIdx.x, s = c & 7, pair = c >> 3, bb = pair & 1, kvh = pair >> 1;
  const int sr = (s + pair) & 7;
  const bool isq = (sr & 3) == 0;
  const int qi = pair * 2 + (sr >> 2), gi = pair * 6 + (sr >> 2) * 3 + (sr & 3) - 1;
  const int G = lane >> 4, cc = lane & 15, sel = (lane >> 3) & 1;
  u32x4 Wt[18];
  const int ww = w - 4;
  const int sl = isq ? qi * 4 + ww : gi * 3 + ww;
  const int r0 = 72 * (sl & 7) + 18 * G;
  const int blk1 = sl >> 3;
  int rstride = isq ? NQ * 2 : F * 2, msel = isq ? 0 : blk1 >= 36 ? 2 : 1, wbytes = isq || ww < 3 ? D * rstride : 0;
  int woff = r0 * rstride + (blk1 - (msel == 2 ? 36 : 0)) * 256 + cc * 16, xoff = lane * 36;
  u32x2 kw;
  asm volatile("s_load_dwordx2 %0, %1, %2" : "=&s"(kw) : "s"(__builtin_amdgcn_kernarg_segment_ptr()), "s"(msel * 8));
  asm volatile("" : "+v"(woff), "+v"(xoff));
  asm volatile("s_waitcnt lgkmcnt(0)" : "+s"(ka), "+s"(kb), "+s"(tst), "+s"(kw));
  { auto P = [](unsigned l, unsigned h) { return (u16*)(((u64)h << 32) | l); };
    a.wq = P(ka[0], ka[1]); a.wg = P(ka[2], ka[3]); a.wu = P(ka[4], ka[5]); a.x = P(ka[6], ka[7]); a.k = P(ka[8], ka[9]); a.v = P(ka[10], ka[11]);
    a.wo = P(ka[12], ka[13]); a.wd = P(ka[14], ka[15]); a.out = P(kb[0], kb[1]); a.ws = (unsigned char*)P(kb[2], kb[3]); }
  const rsrc_t rx = mk(a.x, 2 * D * 2), rw = mk(a.ws, WS_END);
  u32x3 xs[3];
  u32x2 ev;
  if (w >= 4) {
    __builtin_amdgcn_s_setprio(2);
    const rsrc_t r1 = mk((const void*)(((u64)kw.y << 32) | kw.x), wbytes);
    TS(11);
#pragma unroll
    for (int i = 0; i < 18; ++i) { Wt[i] = ld_nt(r1, woff, i * rstride); __builtin_amdgcn_sched_barrier(0); }
  } else if (w == 0) {
#pragma unroll
    for (int i = 0; i < 3; ++i) xs[i] = __builtin_amdgcn_raw_buffer_load_b96(rx, xoff + i * 12, 0, 0);
    ev = __builtin_amdgcn_raw_buffer_load_b64(rw, WS_E, 0, AUX_SC1);
  }
  __builtin_amdgcn_sched_barrier(0);
  const unsigned mhalf = (lane & 1) ? 0xffff0000u : 0xffffu;
  const u32x4 mask = {(lane & 6) == 0 ? mhalf : 0u, (lane & 6) == 2 ? mhalf : 0u, (lane & 6) == 4 ? mhalf : 0u, (lane & 6) == 6 ? mhalf : 0u};
  const int j = bb * 8 + s;
  const int li0 = isq ? 960 + qi * 3 : gi * 5, nq3 = isq ? 3 : 5;
  const int seg = w < 4 ? w : 4;
  const bool segok = seg < 4 || cc < 8;
  const int wo_off = segok ? (kvh * 256 + (j >> 3) * 128 + 8 * (4 * (j & 3) + G) + 4 * ((j >> 2) & 1)) * (D * 2) + seg * 256 + cc * 16 : OOB;
  const int wd_off = segok ? G * (D * 2) + seg * 256 + cc * 16 : OOB;
  const rsrc_t ro = mk(a.wo, NQ * D * 2), rd = mk(a.wd, F * D * 2), rd2 = mk(a.wd, isq ? 0 : F * D * 2);
  u32x4 W3[9];
  auto issue3 = [&](auto kc) {
    constexpr int k = kc.value;
    if constexpr (k < 5) { const int li = li0 + k; W3[4 + k] = ld_nt(k < 3 ? rd : rd2, wd_off, (128 * (li >> 5) + 8 * (li & 15) + 4 * ((li >> 4) & 1)) * (D * 2)); }
    else W3[k - 5] = ld_nt(ro, wo_off, (k - 5) * (D * 2));
    __builtin_amdgcn_sched_barrier(0);
  };

  if (w < 4) {
    asm volatile("s_waitcnt lgkmcnt(0)" : "+s"(tst));
    const unsigned tstart = (unsigned)tst;
    unsigned cpf = 0;
    if (w == 0) {
      if (lane < N_CNT) L[lane] = 0u;
    } else if (w == 1) {
      cpf = __builtin_amdgcn_raw_buffer_load_b32(mk((const void*)(pc0 & ~(u64)255), CODE_BYTES), lane < 5 ? (pair * 5 + lane) * 128 : OOB, 0, 0);
    }
    const size_t kvbase = ((size_t)(bb * 16 + kvh) * SK + s * 512) * DH;
    const rsrc_t rk = mk(a.k + kvbase, 512 * DH * 2), rv = mk(a.v + kvbase, 512 * DH * 2);
    const int voff = w * 1024 + lane * 16;
    asm volatile("s_waitcnt lgkmcnt(0)" ::: "memory");
    __builtin_amdgcn_s_barrier();
    CBAR();
    TS(1);
    u32x4 K[32], V[32], qa, qb;
    float sc[8];
    unsigned pp[8];
    f32x4 acc = {0.f, 0.f, 0.f, 0.f};
    float mx = -INFINITY, lsum = 0.f, fw = 0.f;
    unsigned elapsed = 0;
    constexpr int PV0 = QK0 + 16;
    auto pv = [&](auto jc) { constexpr int i = jc.value;
      acc = gemv4(__builtin_amdgcn_update_dpp(0u, pp[i >> 2], (i & 3) * 0x55, 0xf, 0xf, true), mask, V[i], acc); };
    static_for<64>([&](auto ic) {
      constexpr int n = ic.value;
      if constexpr (n >= WIN && (n - WIN) % LE == 0) elapsed = now32() - tstart;
      if constexpr (n >= WIN && K_LV == 2) throttle2<WIN - 1, WBIG - 1, PA + (((n - (n - WIN) % LE) * PT) >> 4)>(elapsed);
      if constexpr (n >= WIN && K_LV == 4) throttle4<K_W0 - 1, WIN - 1, K_W2 - 1, WBIG - 1, PA + (((n - (n - WIN) % LE) * PT) >> 4), K_DL>(elapsed);
      if constexpr (n < 32) K[n] = ld_nt(rk, voff, n * 4096);
      else V[n - 32] = ld_nt(rv, voff, (n - 32) * 4096);
      if constexpr (n == WIN - 1) {
        if (w == 0) {
          float sm = 0.f;
    #pragma unroll
          for (int i = 0; i < 9; ++i) { const unsigned t = xs[i / 3][i % 3]; sm += lo(t) + hi(t); }
          sm = sum8(sm); sm += dpp<MIR>(sm); sm = sum_x16(sm);
          const float mean = sm * (1.f / D);
          float sv = 0.f;
    #pragma unroll
          for (int i = 0; i < 9; ++i) { const unsigned t = xs[i / 3][i % 3]; const float d0 = lo(t) - mean, d1 = hi(t) - mean; sv = fmaf(d0, d0, fmaf(d1, d1, sv)); }
          sv = sum8(sv); sv += dpp<MIR>(sv); sv = sum_x16(sv);
          const float mb = rbf(mean), sd = rbf(sqrtf(rbf(rbf(sv * (1.f / D)) + 1e-5f))), rs = __builtin_amdgcn_rcpf(sd);
    #pragma unroll
          for (int i = 0; i < 9; ++i) {
            const unsigned t = xs[i / 3][i % 3];
            const unsigned d = pk(lo(t) - mb, hi(t) - mb);
            const float d0 = lo(d), d1 = hi(d);
            float q0 = d0 * rs, q1 = d1 * rs;
            q0 = fmaf(fmaf(-q0, sd, d0), rs, q0);
            q1 = fmaf(fmaf(-q1, sd, d1), rs, q1);
            *(LAS u32x2*)(L + L_XN + lane * 18 + 2 * i) = u32x2{pk(q0, q0), pk(q1, q1)};
          }
          asm volatile("" : "+v"(ev));
          const u64 tag = (((u64)ev.y << 32) | ev.x) + 1;
          if (lane == 0) *(LAS u32x2*)(L + TAG_LO) = u32x2{(unsigned)tag, (unsigned)(tag >> 32)};
          lds_signal(L + F_X);
        }
      }
      if constexpr (n == PQ_STEP || n == PG_STEP || n == 60) { if (w == 0) lds_signal(L + F_V); }
      if constexpr (n == QK0) {
        asm volatile("" :: "v"(cpf));
        TS(2);
        lds_wait<1>(L + F_Q, 1);
        qa = *(LAS u32x4*)(L + L_Q + sel * 64 + cc * 4);
        qb = *(LAS u32x4*)(L + L_Q + (sel ^ 1) * 64 + cc * 4);
        TS(3);
      }
      if constexpr (n >= QK0 && n < PV0 && (n - QK0) % 2 == 0) {
        constexpr int g = (n - QK0) / 2;
        float t[4];
#pragma unroll
        for (int e = 0; e < 4; ++e) {
          float u = 0.f, v = 0.f;
#pragma unroll
          for (int d = 0; d < 4; ++d) { const unsigned kk = K[4 * g + e][d], q0 = qa[d], q1 = qb[d]; u = dot2(kk, q0, u); v = dot2(kk, q1, v); }
          t[e] = u + dpp<ROR8>(v);
          asm volatile("" : "+v"(t[e]));
          t[e] += dpp<HMIR>(t[e]);
          asm volatile("" : "+v"(t[e]));
        }
        const bool o1 = lane & 1, o2 = lane & 2;
        float r0_ = (o1 ? t[1] : t[0]) + dpp<XOR1>(o1 ? t[0] : t[1]);
        asm volatile("" : "+v"(r0_));
        float r1_ = (o1 ? t[3] : t[2]) + dpp<XOR1>(o1 ? t[2] : t[3]);
        const float r = (o2 ? r1_ : r0_) + dpp<XOR2>(o2 ? r0_ : r1_);
        sc[g] = rbf(rbf(r) * 0.08838834764831845f);
        mx = fmaxf(mx, sc[g]);
      }
      if constexpr (n == PV0 - 1) {
        mx = fmaxf(mx, dpp<XOR1>(mx)); mx = fmaxf(mx, dpp<XOR2>(mx));
        mx = max_x32(max_x16(mx));
        const float mneg = -mx * LOG2E;
#pragma unroll
        for (int i = 0; i < 8; ++i) { const float e = __builtin_amdgcn_exp2f(fmaf(sc[i], LOG2E, mneg)); lsum += e; pp[i] = pk(e, e); }
        lsum += dpp<XOR1>(lsum); lsum += dpp<XOR2>(lsum);
        lsum = sum_x32(sum_x16(lsum));
        if ((lane & 0x37) == 0) *(LAS f32x2*)(L + L_ST + w * 4 + sel * 2) = f32x2{mx, lsum};
        lds_signal(L + C_ST);
        TS(4);
      }
      if constexpr (n >= PV0) {
        constexpr int done = (n - PV0) * 4 < n - 32 - WBIG ? (n - PV0) * 4 : n - 32 - WBIG;
        constexpr int upto = (n - PV0 + 1) * 4 < n - 32 - WBIG + 1 ? (n - PV0 + 1) * 4 : n - 32 - WBIG + 1;
        static_for<32>([&](auto jc) { if constexpr (jc.value >= done && jc.value < upto) pv(jc); });
      }
      if constexpr (n == 63) {
        lds_wait<1>(L + C_ST, 4);
        const LAS float* st = (const LAS float*)(L + L_ST + (lane >> 5) * 2);
        fw = __builtin_amdgcn_exp2f((st[w * 4] - fmaxf(fmaxf(st[0], st[4]), fmaxf(st[8], st[12]))) * LOG2E);
      }
      __builtin_amdgcn_sched_barrier(0);
    });
    const unsigned t10 = L[TAG_LO] & TM;
    const int ooff = WS_O + ((kvh * 16 + (lane >> 2)) * 16 + j) * 64 + (lane & 3) * 16;
    static_for<32>([&](auto jc) {
      constexpr int i = jc.value;
      if constexpr (i >= ((63 - PV0 + 1) * 4 < 63 - 32 - WBIG + 1 ? (63 - PV0 + 1) * 4 : 63 - 32 - WBIG + 1)) {
        pv(jc);
        __builtin_amdgcn_sched_barrier(0);
        if constexpr (i < 31 && i >= 31 - TAILN) issue3(IC<i - (31 - TAILN)>{});
      }
    });
    TS(5);
    *(LAS f32x4*)(L + L_D + w * 256 + lane * 4) = acc * fw;
    if (lds_arrive(L + C_D) == 3) {
      f32x4 o = *(LAS f32x4*)(L + L_D + lane * 4);
#pragma unroll
      for (int i = 1; i < 4; ++i) o += *(LAS f32x4*)(L + L_D + i * 256 + lane * 4);
      st_sc(rw, ooff, 0, enc4(o, t10));
      CBAR();
      lds_signal(L + F_OP);
      TS(11);
    } else if (HOLD) lds_wait<0>(L + F_OP, 1);
    if (K_A3) elapsed = now32() - tstart;
    static_for<9>([&](auto kc) {
      if constexpr (kc.value >= TAILN) {
        if constexpr (kc.value >= W3WIN) { if (K_A3) throttle2<W3WIN - 1, 9, K_A3>(elapsed); else throttle<W3WIN - 1>(); }
        issue3(kc);
      }
    });
  } else {
    unsigned tl[10];
    if (K_TLB && ww == 3) {
      const size_t kvb = ((size_t)(bb * 16 + kvh) * SK + s * 512) * DH * 2;
      const int lj = li0 + nq3 - 1;
      const char* pg[10] = {(const char*)a.out, (const char*)a.v + kvb, (const char*)a.k + kvb, (const char*)a.wo + (kvh * 256 + bb * 128 + 32 * (j & 3)) * (D * 2),
                            (const char*)a.wd + (128 * (li0 >> 5) + 8 * (li0 & 15)) * (D * 2), (const char*)a.wd + (128 * (lj >> 5) + 8 * (lj & 15)) * (D * 2),
                            (const char*)a.ws + WS_END - 64, (const char*)a.v + kvb + 65536, (const char*)a.k + kvb + 65536, (const char*)a.ws + 1048576};
#pragma unroll
      for (int i = 0; i < K_TLB; ++i) asm volatile("s_load_dword %0, %1, 0x0" : "=&s"(tl[i]) : "s"(pg[i]));
    }
    __builtin_amdgcn_s_barrier();
    if (K_TLB && ww == 3) {
      asm volatile("s_waitcnt lgkmcnt(0)" ::: "memory");
#pragma unroll
      for (int i = 0; i < K_TLB; ++i) asm volatile("" :: "s"(tl[i]));
    }
    CBAR();
    TS(1);
    lds_wait<2>(L + F_X, 1);
    const unsigned tlo = L[TAG_LO], thi = L[TAG_HI], t10 = tlo & TM;
    if (isq || ww < 3) {
      u32x2 cf[9];
#pragma unroll
      for (int i = 0; i < 9; ++i) cf[i] = *(LAS u32x2*)(L + L_XN + sel * D + r0 + 2 * i);
      f32x4 d0 = {0.f, 0.f, 0.f, 0.f};
#pragma unroll
      for (int i = 0; i < 18; ++i) { const unsigned t = cf[i >> 1][i & 1]; d0 = gemv4(t, mask, Wt[i], d0); }
      *(LAS f32x4*)(L + L_P1 + ww * 256 + lane * 4) = d0;
      lds_signal(L + C_P1);
    }
    TS(2);
    if (ww < 2) {
      const int sl0 = sl - ww, nA = isq ? 4 : min(3, 8 - (sl0 & 7)), i0 = ww ? nA : 0, i1 = ww ? (isq ? 4 : 3) : nA;
      if (i0 < i1) {
        lds_wait<1>(L + C_P1, isq ? 4 : 3);
        f32x4 t = *(LAS f32x4*)(L + L_P1 + i0 * 256 + lane * 4);
        for (int i = i0 + 1; i < i1; ++i) t += *(LAS f32x4*)(L + L_P1 + i * 256 + lane * 4);
        if (isq) {
          const unsigned t8 = tlo & 0xffu;
          st_sc(rw, WS_Q + ((((qi >> 2) * 2 + (lane >> 5)) * 4 + (qi & 3)) * 32 + (lane & 31)) * 16, 0,
                u32x4{((asu(t.x) + 0x80u) & ~0xffu) | t8, ((asu(t.y) + 0x80u) & ~0xffu) | t8, ((asu(t.z) + 0x80u) & ~0xffu) | t8, ((asu(t.w) + 0x80u) & ~0xffu) | t8});
        } else {
          const int blk = (sl0 >> 3) + ww, mat = blk >= 36, rank = gi - (blk * 8) / 3;
          const int li = (blk - 36 * mat) * 32 + (lane & 31);
          const int box = li < 960 ? li / 5 : 192 + (li - 960) / 3, slot = li < 960 ? li - box * 5 : li - 960 - (box - 192) * 3;
          st_sc(rw, WS_GU + box * 1280 + (((mat * 4 + rank) * 2 + (lane >> 5)) * 5 + slot) * 16, 0, enc4(t, t10));
        }
      }
    }
    if (ww == 3 && c < 72 && lane < 4) {
      const int sg = c >> 4, g2 = sg < 4 ? (c >> 2) & 3 : (c >> 1) & 3, mm = sg < 4 ? c & 3 : c & 1;
      *(LAS u32x2*)(L + L_XR + lane * 2) = __builtin_amdgcn_raw_buffer_load_b64(rx, ((g2 >> 1) * D + 128 * sg + 8 * (4 * mm + lane) + 4 * (g2 & 1)) * 2, 0, 0);
    }
    TS(3);
    if (ww == 0) {
      lds_wait<4>(L + F_V, 1);
      u32x4 g[2];
      const int qoff = WS_Q + (kvh * 2 + bb) * 2048 + lane * 16;
#pragma unroll 1
      for (unsigned spin = 0; spin < SPIN_CAP; ++spin) {
        CBAR();
        g[0] = ld_sc(rw, qoff, 0); g[1] = ld_sc(rw, qoff, 1024);
        if (__all(((diff4(g[0], tlo) | diff4(g[1], tlo)) & 0xffu) == 0)) break;
        __builtin_amdgcn_s_sleep(16);
      }
#pragma unroll
      for (int h = 0; h < 2; ++h) {
        const float q0 = sum_x32(asf(g[h].x & ~0xffu)), q1 = sum_x32(asf(g[h].y & ~0xffu)), q2 = sum_x32(asf(g[h].z & ~0xffu)), q3 = sum_x32(asf(g[h].w & ~0xffu));
        if (lane < 32) *(LAS u32x2*)(L + L_Q + h * 64 + cc * 4 + (G & 1) * 2) = u32x2{pk(q0, q1), pk(q2, q3)};
      }
      lds_signal(L + F_Q);
      TS(4);
      const int goff = WS_O + ((kvh * 16 + j) * 16 + cc) * 64 + G * 16, moff = WS_ST + ((kvh * 2 + (j >> 3)) * 16 + cc) * 16;
      const unsigned t16 = tlo & 0xffffu;
      lds_wait<4>(L + C_ST, 4);
      f32x2 st[4];
#pragma unroll
      for (int i = 0; i < 4; ++i) st[i] = *(LAS f32x2*)(L + L_ST + i * 4 + (lane >> 5) * 2);
      const float M = fmaxf(fmaxf(st[0].x, st[1].x), fmaxf(st[2].x, st[3].x));
      float Ls = 0.f;
#pragma unroll
      for (int i = 0; i < 4; ++i) Ls = fmaf(__builtin_amdgcn_exp2f((st[i].x - M) * LOG2E), st[i].y, Ls);
      st_sc(rw, (lane & 31) == 0 ? WS_ST + ((kvh * 2 + (lane >> 5)) * 16 + j) * 16 : OOB, 0,
            u32x4{(asu(M) & 0xffff0000u) | t16, enc(Ls, t10), tlo, thi});
      TS(5);
      if (K_STS) lds_wait<4>(L + F_V, 3); else lds_wait<1>(L + C_D, 1);
      u32x4 ov, ml;
#pragma unroll 1
      for (unsigned spin = 0; spin < SPIN_CAP; ++spin) {
        CBAR();
        ml = ld_sc(rw, moff, 0);
        if (__all((((ml.y ^ t10) & TM) | ((ml.x ^ t16) & 0xffffu) | (ml.z ^ tlo) | (ml.w ^ thi)) == 0)) break;
        if (K_STS > 1) __builtin_amdgcn_s_sleep(K_STS);
      }
      const float ms = hi(ml.x), e = __builtin_amdgcn_exp2f((ms - max8(ms)) * LOG2E);
      const float f = e * __builtin_amdgcn_rcpf(sum8(e * asf(ml.y & ~TM)));
      lds_wait<0>(L + F_OP, 1);
      if (K_FW) lds_wait<0>(L + F_W, K_FW);
      if (K_OG) { asm volatile("s_waitcnt lgkmcnt(0)" : "+s"(tst)); while (now32() - (unsigned)tst < K_OG) {} }
      TS(6);
#pragma unroll 1
      for (unsigned spin = 0; spin < SPIN_CAP; ++spin) {
        CBAR();
        ov = ld_sc(rw, goff, 0);
        if (__all((diff4(ov, t10) & TM) == 0)) break;
      }
      const f32x4 od = dec4(ov);
      const float o0 = sum8(f * od.x), o1 = sum8(f * od.y), o2 = sum8(f * od.z), o3 = sum8(f * od.w);
      if ((lane & 7) == 0) *(LAS u32x4*)(L + L_CO + (lane >> 3) * 4) = u32x4{pk(o0, o0), pk(o1, o1), pk(o2, o2), pk(o3, o3)};
      lds_signal(L + F_O);
      TS(7);
    } else if (ww == 1) {
      lds_wait<4>(L + F_V, 2);
      u32x4 p[2];
      int off[2];
#pragma unroll
      for (int i = 0; i < 2; ++i) {
        const int it = i * 16 + (lane >> 2), m2 = it >= 10, rem = it - 10 * m2, qd = rem >= 5 ? rem - 5 : rem, cb = (li0 + qd) >> 5;
        const bool ok = it < 20 && qd < nq3 && ((lane & 3) < 3 || cb % 3 == 1);
        off[i] = ok ? WS_GU + (isq ? 192 + qi : gi) * 1280 + ((m2 * 4 + (lane & 3)) * 10 + rem) * 16 : OOB;
      }
#pragma unroll 1
      for (unsigned spin = 0; spin < SPIN_CAP; ++spin) {
        CBAR();
        p[0] = ld_sc(rw, off[0], 0); p[1] = ld_sc(rw, off[1], 0);
        if (__all((((off[0] != OOB ? diff4(p[0], t10) : 0u) | (off[1] != OOB ? diff4(p[1], t10) : 0u)) & TM) == 0)) break;
        __builtin_amdgcn_s_sleep(16);
      }
      TS(4);
#pragma unroll
      for (int i = 0; i < 2; ++i) {
        f32x4 d = dec4(p[i]);
#pragma unroll
        for (int k = 0; k < 4; ++k) { float u = d[k]; u += dpp<XOR1>(u); d[k] = u + dpp<XOR2>(u); }
        if ((lane & 3) == 0 && (i == 0 || lane < 16)) *(LAS f32x4*)(L + L_GU + (i * 16 + (lane >> 2)) * 4) = d;
      }
      __builtin_amdgcn_fence(__ATOMIC_ACQ_REL, "wavefront", "local");
      if (lane < 40) {
        const float h = rbf(asf(L[L_GU + lane])) * rbf(asf(L[L_GU + 40 + lane]));
        const int b = lane >= 20, qr = lane - 20 * b;
        L[L_CH + b * 32 + (qr & 3) * 8 + (qr >> 2)] = pk(h, h);
      }
      lds_signal(L + F_H);
      TS(5);
      lds_wait<2>(L + C_D, 1);
      static_for<9>([&](auto kc) { issue3(kc); });
    }
  }

  const unsigned tg = L[TAG_LO] & TM;
  if (w < 4 || w == 5) {
    const int yoff = segok ? WS_Y + ((seg * 16 + (seg < 4 ? (lane >> 2) : G * 2 + (cc >> 2))) * 256 + c) * 64 + (lane & 3) * 16 : OOB;
    f32x4 y = {0.f, 0.f, 0.f, 0.f};
    lds_wait<1>(L + F_H, 1);
    const u32x4 ch = *(LAS u32x4*)(L + L_CH + sel * 32 + G * 8);
    const unsigned ch4 = L[L_CH + sel * 32 + G * 8 + 4];
#pragma unroll
    for (int i = 0; i < 5; ++i) { const unsigned t = i < 4 ? ch[i & 3] : ch4; y = gemv4(t, mask, W3[4 + i], y); }
    if (K_FW) { asm volatile("" : "+v"(y)); lds_signal(L + F_W); }
    TS(6);
    lds_wait<0>(L + F_O, 1);
    const u32x4 co = *(LAS u32x4*)(L + L_CO + (lane >> 3) * 4);
    TS(7);
#pragma unroll
    for (int i = 0; i < 4; ++i) { const unsigned t = co[i]; y = gemv4(t, mask, W3[i], y); }
    st_sc(rw, yoff, 0, enc4(y, tg));
    TS(8);
  } else if (c < 72) lds_wait<2>(L + F_O, 1);

  if (c < 72) {
    __builtin_amdgcn_s_sleep(FIN_DELAY);
    u32x4 p0, p1;
    const int off = WS_Y + c * 16384 + w * 2048 + lane * 16;
#pragma unroll 1
    for (unsigned spin = 0; spin < SPIN_CAP; ++spin) {
      CBAR();
      p0 = ld_sc(rw, off, 0); p1 = ld_sc(rw, off, 1024);
      if (__all(((diff4(p0, tg) | diff4(p1, tg)) & TM) == 0)) break;
    }
    TS(9);
    f32x4 t = dec4(p0) + dec4(p1);
#pragma unroll
    for (int i = 0; i < 4; ++i) { float u = t[i]; u += dpp<ROR4>(u); u += dpp<ROR8>(u); t[i] = sum_x32(sum_x16(u)); }
    if (lane < 4) *(LAS f32x4*)(L + L_FIN + w * 16 + lane * 4) = t;
    if (lds_arrive(L + C_FIN) == 7 && lane < 4) {
      f32x4 r = *(LAS f32x4*)(L + L_FIN + lane * 4);
#pragma unroll
      for (int i = 1; i < 8; ++i) r += *(LAS f32x4*)(L + L_FIN + i * 16 + lane * 4);
      const u32x2 xr = *(LAS u32x2*)(L + L_XR + lane * 2);
      const int sg = c >> 4, g2 = sg < 4 ? (c >> 2) & 3 : (c >> 1) & 3, mm = sg < 4 ? c & 3 : c & 1;
      __builtin_amdgcn_raw_buffer_store_b64(u32x2{pk(lo(xr.x) + r.x, hi(xr.x) + r.y), pk(lo(xr.y) + r.z, hi(xr.y) + r.w)}, mk(a.out, 2 * D * 2),
                                            ((g2 >> 1) * D + 128 * sg + 8 * (4 * mm + lane) + 4 * (g2 & 1)) * 2, 0, 0);
      if (c == 0 && lane == 0) __builtin_amdgcn_raw_buffer_store_b64(*(LAS u32x2*)(L + TAG_LO), rw, WS_E, 0, AUX_SC1);
    }
    TS(10);
  }
  TSFLUSH();
}

}

extern "C" void kernel_launch(void* const* d_in, const int* in_sizes, int n_in, void* d_out, int out_size, void* d_ws, size_t ws_size,
                              hipStream_t stream) {
  Args a;
  a.x = (const u16*)d_in[0]; a.k = (const u16*)d_in[1]; a.v = (const u16*)d_in[2]; a.wq = (const u16*)d_in[3]; a.wo = (const u16*)d_in[4];
  a.wg = (const u16*)d_in[6]; a.wu = (const u16*)d_in[7]; a.wd = (const u16*)d_in[8];
  a.out = (u16*)d_out; a.ws = (unsigned char*)d_ws;
  fused_decode<<<NWG, NTHR, 0, stream>>>(a);
}
```
